```python
import torch

def _softmax(x, axis=-1):
    return torch.softmax(x, dim=axis)

def _gelu(x):
    return torch.nn.functional.gelu(x, approximate='tanh')

def _silu(x):
    return torch.nn.functional.silu(x)

def _sigmoid(x):
    return torch.sigmoid(x)

def _layernorm(x, axis=-1, eps=1e-05):
    m = x.mean(dim=axis, keepdim=True)
    v = x.var(dim=axis, keepdim=True, unbiased=False)
    return (x - m) / torch.sqrt(v + eps)

def _scatter_add(tgt, idx, src, axis=0):
    return tgt.clone().index_add_(axis, idx, src)

def _segment_sum(src, seg, n):
    out = torch.zeros((n,) + src.shape[1:], dtype=src.dtype)
    return out.index_add_(0, seg, src)

def _pad(x, pw):
    flat = tuple((v for pair in reversed(pw) for v in pair))
    return torch.nn.functional.pad(x, flat)
kernel_name = 'adaLN_cond__linear_kernel_hedgehog__mixer_gmlp__prompt'

def setup_inputs():

    _w = lambda a: (lambda t: t.to(torch.bfloat16) if t.is_floating_point() else t)(torch.as_tensor(a))

    def _A_setup():
        B, S, D, H = (1, 2048, 1024, 32)
        Dh = D // H
        return {'x': _w((lambda *s: torch.randn(*s, dtype=torch.bfloat16) * 0.05)(B, S, D)), 'wqkv': _w((lambda *s: torch.randn(*s, dtype=torch.bfloat16) * 0.4)(D, 3 * D)), 'w_phi': _w((lambda *s: torch.randn(*s, dtype=torch.bfloat16) * 0.4)(H, Dh, Dh) * Dh ** (-0.5)), 'wo': _w((lambda *s: torch.randn(*s, dtype=torch.bfloat16) * 0.05)(D, D))}
    _A_in = _A_setup()

    def _B_setup():
        B, S, D, F = (1, 2048, 1024, 3584)
        Fh = F // 2
        return {'x': _w((lambda *s: torch.randn(*s, dtype=torch.bfloat16) * 0.05)(B, S, D)), 'w_in': _w((lambda *s: torch.randn(*s, dtype=torch.bfloat16) * 0.2)(D, 2 * Fh)), 'w_sp': _w(0.01 * (lambda *s: torch.randn(*s, dtype=torch.bfloat16) * 0.2)(S, S)), 'b_sp': _w(1.0 + 0.01 * (lambda *s: torch.randn(*s, dtype=torch.bfloat16) * 0.05)(S)), 'w_out': _w((lambda *s: torch.randn(*s, dtype=torch.bfloat16) * 0.05)(Fh, D))}
    _B_in = _B_setup()
    return {'A_x': _A_in['x'], 'A_wqkv': _A_in['wqkv'], 'A_w_phi': _A_in['w_phi'], 'A_wo': _A_in['wo'], 'B_x': _B_in['x'], 'B_w_in': _B_in['w_in'], 'B_w_sp': _B_in['w_sp'], 'B_b_sp': _B_in['b_sp'], 'B_w_out': _B_in['w_out'], 'c': _w((lambda *s: torch.randn(*s, dtype=torch.bfloat16) * 0.1)(1, 1024) * 8.0), 'w_mod': _w((lambda *s: torch.randn(*s, dtype=torch.bfloat16) * 0.1)(1024, 6 * 1024) * 1024 ** (-0.5) * 8.0)}

def reference(A_x, A_wqkv, A_w_phi, A_wo, B_x, B_w_in, B_w_sp, B_b_sp, B_w_out, c, w_mod):

    def _A_ref(x, wqkv, w_phi, wo):
        B, S, D = x.shape
        H = 32
        Dh = D // H
        qkv = (lambda x, *d: x.permute(*d))((x @ wqkv).reshape(B, S, 3, H, Dh), 2, 0, 3, 1, 4)
        q, k, v = (qkv[0], qkv[1], qkv[2])
        pq = torch.einsum('bhsd,hdf->bhsf', q, w_phi)
        pk = torch.einsum('bhsd,hdf->bhsf', k, w_phi)
        phi_q = torch.concatenate([_softmax(pq, axis=-1), _softmax(-pq, axis=-1)], axis=-1)
        phi_k = torch.concatenate([_softmax(pk, axis=-1), _softmax(-pk, axis=-1)], axis=-1)
        kv = torch.einsum('bhsf,bhsd->bhfd', phi_k, v)
        z = torch.einsum('bhsf->bhf', phi_k)
        num = torch.einsum('bhqf,bhfd->bhqd', phi_q, kv)
        den = torch.einsum('bhqf,bhf->bhq', phi_q, z)[..., None] + 1e-06
        o = num / den
        return (lambda x, *d: x.permute(*d))(o, 0, 2, 1, 3).reshape(B, S, D) @ wo

    def _B_ref(x, w_in, w_sp, b_sp, w_out):
        B, S, D = x.shape
        F = w_in.shape[-1]
        Fh = F // 2
        z = _gelu(torch.einsum('bsd,df->bsf', x, w_in))
        z1 = z[..., :Fh].clone()
        z2 = _layernorm(z[..., Fh:], axis=-1)
        g = torch.einsum('ts,bsf->btf', w_sp, z2) + b_sp[None, :, None]
        return torch.einsum('bsf,fd->bsd', z1 * g, w_out)
    x = A_x
    mod = (_silu(c) @ w_mod).reshape(c.shape[0], 6, 1, -1)
    sa, ca, ga, sb, cb, gb = (mod[:, 0].clone(), mod[:, 1].clone(), mod[:, 2].clone(), mod[:, 3].clone(), mod[:, 4].clone(), mod[:, 5].clone())
    h = _layernorm(x, axis=-1) * (1 + ca) + sa
    x = x + ga * _A_ref(h, A_wqkv, A_w_phi, A_wo)
    h = _layernorm(x, axis=-1) * (1 + cb) + sb
    x = x + gb * _B_ref(h, B_w_in, B_w_sp, B_b_sp, B_w_out)
    return x
```

```cpp
#include <hip/hip_runtime.h>
#include <hip/hip_bf16.h>
#include <stdint.h>
typedef unsigned short u16;
__device__ __forceinline__ float bf2f(u16 v){ return __uint_as_float(((unsigned)v)<<16); }
__device__ __forceinline__ u16 f2bf(float f){ unsigned u=__float_as_uint(f); u += 0x7fffu + ((u>>16)&1u); return (u16)(u>>16); }

constexpr int S=2048, D=1024, H=32, DH=32, F=3584, FH=1792;

__global__ void k_mod(const u16* c, const u16* wmod, float* mod){
  int n = blockIdx.x*blockDim.x+threadIdx.x;
  float acc=0.f;
  for(int k=0;k<D;++k){ float x=bf2f(c[k]); float s=x/(1.f+__expf(-x)); s=bf2f(f2bf(s)); acc += s*bf2f(wmod[(size_t)k*6144+n]); }
  mod[n]=acc;
}
template<typename T> __device__ float ld(const T* p, size_t i);
template<> __device__ float ld<u16>(const u16* p, size_t i){ return bf2f(p[i]); }
template<> __device__ float ld<float>(const float* p, size_t i){ return p[i]; }
template<typename T>
__global__ void k_ln(const T* x, int ldx, int n, const float* shift, const float* scale, u16* h, int ldh){
  int row=blockIdx.x; __shared__ float red[256];
  float s=0; for(int i=threadIdx.x;i<n;i+=256) s+=ld<T>(x,(size_t)row*ldx+i);
  red[threadIdx.x]=s; __syncthreads();
  for(int o=128;o>0;o>>=1){ if(threadIdx.x<o) red[threadIdx.x]+=red[threadIdx.x+o]; __syncthreads(); }
  float mean=red[0]/n; __syncthreads();
  float q=0; for(int i=threadIdx.x;i<n;i+=256){ float d=ld<T>(x,(size_t)row*ldx+i)-mean; q+=d*d; }
  red[threadIdx.x]=q; __syncthreads();
  for(int o=128;o>0;o>>=1){ if(threadIdx.x<o) red[threadIdx.x]+=red[threadIdx.x+o]; __syncthreads(); }
  float rstd=rsqrtf(red[0]/n+1e-5f);
  for(int i=threadIdx.x;i<n;i+=256){ float v=(ld<T>(x,(size_t)row*ldx+i)-mean)*rstd; if(scale) v=v*(1.f+scale[i])+shift[i]; h[(size_t)row*ldh+i]=f2bf(v); }
}
__global__ void k_gemm(const u16* A,int lda,const u16* B,int ldb,float* C,int ldc,int K){
  __shared__ float As[16][65], Bs[16][65];
  int tx=threadIdx.x&15, ty=threadIdx.x>>4; int m0=blockIdx.y*64, n0=blockIdx.x*64;
  float acc[4][4]={};
  for(int k0=0;k0<K;k0+=16){
    for(int i=threadIdx.x;i<1024;i+=256){ int r=i>>4,kk=i&15; As[kk][r]=bf2f(A[(size_t)(m0+r)*lda+k0+kk]); int kb=i>>6,cn=i&63; Bs[kb][cn]=bf2f(B[(size_t)(k0+kb)*ldb+n0+cn]); }
    __syncthreads();
    for(int kk=0;kk<16;++kk){ float a[4],b[4]; for(int i=0;i<4;++i){a[i]=As[kk][ty*4+i]; b[i]=Bs[kk][tx*4+i];} for(int i=0;i<4;++i)for(int j=0;j<4;++j) acc[i][j]+=a[i]*b[j]; }
    __syncthreads();
  }
  for(int i=0;i<4;++i)for(int j=0;j<4;++j) C[(size_t)(m0+ty*4+i)*ldc+n0+tx*4+j]=acc[i][j];
}
__global__ void k_phi(const float* qkv, const u16* wphi, float* phiq, float* phik){
  int idx=blockIdx.x*blockDim.x+threadIdx.x;
  int which=idx/(S*H); int r=idx%(S*H); int s=r/H,h=r%H;
  const float* q=qkv+(size_t)s*3*D+which*D+h*DH;
  float p[32]; float mx=-1e30f,mn=1e30f;
  for(int f=0;f<32;++f){ float a=0; for(int d=0;d<32;++d) a+=bf2f(f2bf(q[d]))*bf2f(wphi[h*1024+d*32+f]); p[f]=a; mx=fmaxf(mx,a); mn=fminf(mn,a);}
  float s1=0,s2=0; for(int f=0;f<32;++f){ s1+=__expf(p[f]-mx); s2+=__expf(mn-p[f]); }
  float* o=(which?phik:phiq)+((size_t)h*S+s)*64;
  for(int f=0;f<32;++f){ o[f]=__expf(p[f]-mx)/s1; o[32+f]=__expf(mn-p[f])/s2; }
}
__global__ void k_kv(const float* qkv,const float* phik,float* kv){
  int h=blockIdx.x;
  for(int i=threadIdx.x;i<64*33;i+=blockDim.x){ int f=i/33,d=i%33; float a=0; for(int s=0;s<S;++s){ float v= d<32? qkv[(size_t)s*3*D+2*D+h*DH+d]:1.f; a+=phik[((size_t)h*S+s)*64+f]*v; } kv[h*64*33+i]=a; }
}
__global__ void k_o(const float* phiq,const float* kv,u16* o){
  int idx=blockIdx.x*blockDim.x+threadIdx.x;
  int s=idx/D, c=idx%D, h=c/32,d=c%32; float num=0,den=0;
  for(int f=0;f<64;++f){ float p=phiq[((size_t)h*S+s)*64+f]; num+=p*kv[h*64*33+f*33+d]; den+=p*kv[h*64*33+f*33+32]; }
  o[idx]=f2bf(num/(den+1e-6f));
}
__global__ void k_res1(const u16* x,const float* a,const float* g,float* x1){ int idx=blockIdx.x*blockDim.x+threadIdx.x; x1[idx]=bf2f(x[idx])+g[idx%D]*a[idx]; }
__global__ void k_gelu(float* z){ size_t idx=(size_t)blockIdx.x*blockDim.x+threadIdx.x; float v=z[idx]; float t=tanhf(0.7978845608f*(v+0.044715f*v*v*v)); z[idx]=0.5f*v*(1.f+t); }
__global__ void k_gate(const float* z,const float* g,const u16* b,u16* y){ size_t idx=(size_t)blockIdx.x*blockDim.x+threadIdx.x; int s=idx/FH,f=idx%FH; y[idx]=f2bf(z[(size_t)s*F+f]*(g[idx]+bf2f(b[s]))); }
__global__ void k_res2(const float* x1,const float* a,const float* g,u16* out){ int idx=blockIdx.x*blockDim.x+threadIdx.x; out[idx]=f2bf(x1[idx]+g[idx%D]*a[idx]); }

extern "C" void kernel_launch(void* const* d_in, const int* in_sizes, int n_in, void* d_out, int out_size, void* d_ws, size_t ws_size, hipStream_t stream){
  const u16* x=(const u16*)d_in[0]; const u16* wqkv=(const u16*)d_in[1]; const u16* wphi=(const u16*)d_in[2]; const u16* wo=(const u16*)d_in[3];
  const u16* win=(const u16*)d_in[5]; const u16* wsp=(const u16*)d_in[6]; const u16* bsp=(const u16*)d_in[7]; const u16* wout=(const u16*)d_in[8];
  const u16* c=(const u16*)d_in[9]; const u16* wmod=(const u16*)d_in[10];
  char* w=(char*)d_ws; size_t off=0; auto al=[&](size_t b){ void* p=w+off; off+=(b+255)&~(size_t)255; return p; };
  float* mod=(float*)al(6144*4); u16* h=(u16*)al(S*D*2); float* qkv=(float*)al((size_t)S*3*D*4); float* phiq=(float*)al((size_t)H*S*64*4); float* phik=(float*)al((size_t)H*S*64*4);
  float* kv=(float*)al(H*64*33*4); u16* o=(u16*)al(S*D*2); float* a=(float*)al(S*D*4); float* x1=(float*)al(S*D*4); float* z=(float*)al((size_t)S*F*4); u16* z2=(u16*)al((size_t)S*FH*2);
  float* g=(float*)al((size_t)S*FH*4); u16* y=(u16*)al((size_t)S*FH*2);
  k_mod<<<6144/256,256,0,stream>>>(c,wmod,mod);
  k_ln<u16><<<S,256,0,stream>>>(x,D,D,mod,mod+D,h,D);
  k_gemm<<<dim3(3*D/64,S/64),256,0,stream>>>(h,D,wqkv,3*D,qkv,3*D,D);
  k_phi<<<S*H*2/256,256,0,stream>>>(qkv,wphi,phiq,phik);
  k_kv<<<H,1024,0,stream>>>(qkv,phik,kv);
  k_o<<<S*D/256,256,0,stream>>>(phiq,kv,o);
  k_gemm<<<dim3(D/64,S/64),256,0,stream>>>(o,D,wo,D,a,D,D);
  k_res1<<<S*D/256,256,0,stream>>>(x,a,mod+2*D,x1);
  k_ln<float><<<S,256,0,stream>>>(x1,D,D,mod+3*D,mod+4*D,h,D);
  k_gemm<<<dim3(F/64,S/64),256,0,stream>>>(h,D,win,F,z,F,D);
  k_gelu<<<(size_t)S*F/256,256,0,stream>>>(z);
  k_ln<float><<<S,256,0,stream>>>(z+FH,F,FH,nullptr,nullptr,z2,FH);
  k_gemm<<<dim3(FH/64,S/64),256,0,stream>>>(wsp,S,z2,FH,g,FH,S);
  k_gate<<<(size_t)S*FH/256,256,0,stream>>>(z,g,bsp,y);
  k_gemm<<<dim3(D/64,S/64),256,0,stream>>>(y,FH,wout,D,a,D,FH);
  k_res2<<<S*D/256,256,0,stream>>>(x1,a,mod+5*D,(u16*)d_out);
}
```

```cpp
#include <hip/hip_runtime.h>
#include <stdint.h>
#include <type_traits>

typedef unsigned short u16;
typedef unsigned long long u64;
typedef short s16x4 __attribute__((ext_vector_type(4)));
typedef short s16x8 __attribute__((ext_vector_type(8)));
typedef __bf16 bf16x8 __attribute__((ext_vector_type(8)));
typedef __bf16 bf16x2 __attribute__((ext_vector_type(2)));
typedef float f32x2 __attribute__((ext_vector_type(2)));
typedef float f32x4 __attribute__((ext_vector_type(4)));
typedef unsigned u32x2 __attribute__((ext_vector_type(2)));
typedef unsigned u32x4 __attribute__((ext_vector_type(4)));
typedef int i32x4 __attribute__((ext_vector_type(4)));
typedef __amdgpu_buffer_rsrc_t rsrc_t;
#define LAS __attribute__((address_space(3)))
#define DI __device__ __forceinline__
#define SB() __builtin_amdgcn_sched_barrier(0)
template <int V> using IC = std::integral_constant<int, V>;
template <int I, int N, class Fn> DI void static_for(Fn&& f) { if constexpr (I < N) { f(IC<I>{}); static_for<I + 1, N>(f); } }

constexpr int S = 2048, D = 1024, FF = 3584, FH = 1792;
constexpr int GM = 4;
constexpr unsigned WS_EPOCH = 0;
constexpr unsigned WS_FLAG = 4096;
enum { F_H = 0, F_O, F_X, F_Y, F_KV, F_KS, F_V, F_ZS, F_Z, F_M  , F_N = F_M + 1 };
constexpr unsigned WS_G0 = WS_FLAG + F_N * 16 * 1024;
constexpr unsigned WS_MP = WS_G0 + 8192;
constexpr unsigned KVB = 4224 * 4;
constexpr unsigned WS_KVP = WS_MP + 64 * 8 * 64 * 4;
constexpr unsigned WS_KVS = WS_KVP + 256 * KVB;
constexpr unsigned WS_VP = WS_KVS + 16 * KVB;
constexpr unsigned WS_XS = WS_VP + 256 * 448 * 4;
constexpr unsigned WS_ZS = WS_XS + 256 * 1024;
constexpr unsigned WS_HP = (WS_ZS + 256 * 1024 + 65535) / 65536 * 65536;
constexpr unsigned WS_OP = WS_HP + S * D * 2;
constexpr unsigned WS_XP = WS_OP + S * D * 2;
constexpr unsigned WS_ZP = WS_XP + S * D * 2;
constexpr unsigned WS_YP = WS_ZP + S * FH * 2;
constexpr unsigned WS_END = WS_YP + S * FH * 2;
constexpr unsigned WS_STAMP = WS_END;
constexpr int SLOT = 16384;
constexpr int L_A = 0, L_B = 4 * SLOT, L_STG = 8 * SLOT, L_TAB = L_STG + 28672, L_TOTAL = 163840;
constexpr int T_RS = L_TAB, T_V1 = L_TAB + 1024, T_V2 = T_V1 + 896, T_GB = L_TAB + 2816, T_X = L_TAB + 3072;
constexpr int G_SC = L_STG + 16384;
constexpr int G_RED = L_STG + 20480;
constexpr int G_CS = L_STG + 21504;
constexpr int G_KVF = L_STG + 17408;
constexpr int G_KT = L_STG + 16384;
static_assert(T_X + 1024 == L_TOTAL, "LDS map");

struct Params {
  const u16* x; const u16* wqkv; const u16* wphi; const u16* wo; const u16* win; const u16* wsp; const u16* bsp;
  const u16* wout; const u16* c; const u16* wmod; u16* out; char* ws;
};

DI i32x4 mk_srd(const void* p, unsigned bytes) {
  i32x4 r; const u64 a = (u64)p;
  r[0] = __builtin_amdgcn_readfirstlane((int)(unsigned)a);
  r[1] = __builtin_amdgcn_readfirstlane((int)((unsigned)(a >> 32) & 0xffffu));
  r[2] = __builtin_amdgcn_readfirstlane((int)bytes);
  r[3] = 0x00020000;
  asm volatile("s_nop 4" : "+s"(r));
  return r;
}
DI rsrc_t mk_rsrc(const void* p, unsigned bytes) { return __builtin_amdgcn_make_buffer_rsrc((void*)p, 0, bytes, 0x00020000); }
template <int OFF, bool SC1> DI void dma16(i32x4 srd, unsigned voff, unsigned soff) {
  if constexpr (SC1) asm volatile("buffer_load_dwordx4 %0, %1, %2 offen offset:%c3 sc1 lds" ::"v"(voff), "s"(srd), "s"(soff), "i"(OFF) : "memory");
  else asm volatile("buffer_load_dwordx4 %0, %1, %2 offen offset:%c3 lds" ::"v"(voff), "s"(srd), "s"(soff), "i"(OFF) : "memory");
}
template <int IMM> DI void set_m0(unsigned base) { asm volatile("s_add_u32 m0, %0, %c1\n\ts_nop 0" ::"s"(base), "i"(IMM) : "memory", "scc"); }
template <int N> DI void wait_vm() { asm volatile("s_waitcnt vmcnt(%0)" ::"n"(N) : "memory"); }
DI void wait_lgkm0() { asm volatile("" ::: "memory"); __builtin_amdgcn_s_waitcnt(0xC07F); asm volatile("" ::: "memory"); }
DI void bar() { asm volatile("s_barrier" ::: "memory"); }
DI void lbar() { wait_lgkm0(); bar(); }
template <int N> DI void bars() { static_for<0, N>([&](auto) { bar(); }); }
DI s16x4 trread(unsigned a) { return __builtin_amdgcn_ds_read_tr16_b64_v4i16((LAS s16x4*)a); }
template <class T> DI T lds_ld(unsigned a) { return *(LAS T*)a; }
template <class T> DI void lds_st(unsigned a, T v) { *(LAS T*)a = v; }
DI f32x4 mfma16(s16x8 a, s16x8 b, f32x4 c) { return __builtin_amdgcn_mfma_f32_16x16x32_bf16(__builtin_bit_cast(bf16x8, a), __builtin_bit_cast(bf16x8, b), c, 0, 0, 0); }
DI unsigned pk2(float a, float b) { f32x2 v = {a, b}; return __builtin_bit_cast(unsigned, __builtin_convertvector(v, bf16x2)); }
DI u32x2 pk4(f32x4 v) { u32x2 r = {pk2(v[0], v[1]), pk2(v[2], v[3])}; return r; }
DI s16x8 pk8(f32x4 a, f32x4 b) { u32x4 r = {pk2(a[0], a[1]), pk2(a[2], a[3]), pk2(b[0], b[1]), pk2(b[2], b[3])}; return __builtin_bit_cast(s16x8, r); }
DI float blo(unsigned w) { return __uint_as_float(w << 16); }
DI float bhi(unsigned w) { return __uint_as_float(w & 0xffff0000u); }
DI float bfr(float v) { return blo(pk2(v, v)); }
DI float x16_add(float v) { auto r = __builtin_amdgcn_permlane16_swap(__float_as_uint(v), __float_as_uint(v), false, false); return __uint_as_float(r[0]) + __uint_as_float(r[1]); }
DI float x32_add(float v) { auto r = __builtin_amdgcn_permlane32_swap(__float_as_uint(v), __float_as_uint(v), false, false); return __uint_as_float(r[0]) + __uint_as_float(r[1]); }
DI float x16_max(float v) { auto r = __builtin_amdgcn_permlane16_swap(__float_as_uint(v), __float_as_uint(v), false, false); return fmaxf(__uint_as_float(r[0]), __uint_as_float(r[1])); }
DI float x32_max(float v) { auto r = __builtin_amdgcn_permlane32_swap(__float_as_uint(v), __float_as_uint(v), false, false); return fmaxf(__uint_as_float(r[0]), __uint_as_float(r[1])); }
DI float x16_min(float v) { auto r = __builtin_amdgcn_permlane16_swap(__float_as_uint(v), __float_as_uint(v), false, false); return fminf(__uint_as_float(r[0]), __uint_as_float(r[1])); }
DI float x32_min(float v) { auto r = __builtin_amdgcn_permlane32_swap(__float_as_uint(v), __float_as_uint(v), false, false); return fminf(__uint_as_float(r[0]), __uint_as_float(r[1])); }
template <int CTRL> DI float dpp_add(float v) { return v + __uint_as_float(__builtin_amdgcn_update_dpp(0u, __float_as_uint(v), CTRL, 0xf, 0xf, true)); }
DI float row16_sum(float v) { v = dpp_add<0xB1>(v); v = dpp_add<0x4E>(v); v = dpp_add<0x141>(v); v = dpp_add<0x140>(v); return v; }
DI float wave_sum(float v) { return x32_add(x16_add(row16_sum(v))); }
DI u64 now() { return __builtin_amdgcn_s_memrealtime(); }
constexpr u64 DEADLINE = 3000000ull;

struct Ctx {
  rsrc_t ws; u32x2 tag; u64 t0; int lane, wid, tm, tn; unsigned lds0;
};
DI void set_flag(const Ctx& c, int type, int group, int idx) {
  if (c.lane == 0) __builtin_amdgcn_raw_buffer_store_b64(c.tag, c.ws, (unsigned)(WS_FLAG + (type * 16 + group) * 1024 + idx * 8), 0, 16);
}
DI bool flags_ok(const Ctx& c, u32x2 v, bool active) { return __all((!active) || (v[0] == c.tag[0] && v[1] == c.tag[1])); }
DI u32x2 flag_ld(const Ctx& c, unsigned voff) { return __builtin_amdgcn_raw_buffer_load_b64(c.ws, voff, 0, 16); }
DI void wait_flags(const Ctx& c, unsigned voff, bool active) {
  for (;;) {
    const u32x2 v = flag_ld(c, voff);
    if (flags_ok(c, v, active)) break;
    if (now() - c.t0 > DEADLINE) break;
    __builtin_amdgcn_s_sleep(1);
  }
  asm volatile("" ::: "memory");
}
DI unsigned flag_off(int type, int group, int idx) { return (unsigned)(WS_FLAG + (type * 16 + group) * 1024 + idx * 8); }

#define STAMP(i) do { } while (0)

struct Src { i32x4 srd; unsigned voff[4]; unsigned base, pstep, kstep; int rot; };
template <int G, int NT, bool SC1> DI void issue(const Src& s, unsigned ring, int kt) {
  int kk = kt + s.rot; kk = kk >= NT ? kk - NT : kk;
  const unsigned so = s.base + (unsigned)kk * s.kstep, lb = ring + (unsigned)(kt & 3) * SLOT;
  static_for<0, (G + 3) / 4>([&](auto q) {
    constexpr int qi = q.value; const unsigned s2 = so + qi * s.pstep;
    set_m0<qi * 4096>(lb);
    dma16<0, SC1>(s.srd, s.voff[0], s2);
    dma16<1024, SC1>(s.srd, s.voff[1], s2);
    if constexpr (qi * 4 + 2 < G) { dma16<2048, SC1>(s.srd, s.voff[2], s2); dma16<3072, SC1>(s.srd, s.voff[3], s2); }
  });
}
template <int G, int NT, bool SC1> DI void issue4(const Src& s, unsigned ring) { issue<G, NT, SC1>(s, ring, 0); issue<G, NT, SC1>(s, ring, 1); issue<G, NT, SC1>(s, ring, 2); issue<G, NT, SC1>(s, ring, 3); }
template <int G, int NT, bool SC1, int GN, int NTN, bool SC1N> DI void load_pass(const Src& s, const Src& n, unsigned ring) {
  wait_vm<3 * G>(); bar();
  for (int k = 0; k < NT - 4; ++k) { wait_vm<2 * G>(); bar(); issue<G, NT, SC1>(s, ring, k + 4); }
  wait_vm<2 * G>(); bar(); if constexpr (GN > 0) issue<GN, NTN, SC1N>(n, ring, 0);
  wait_vm<G + GN>(); bar(); if constexpr (GN > 0) issue<GN, NTN, SC1N>(n, ring, 1);
  wait_vm<2 * GN>(); bar(); if constexpr (GN > 0) issue<GN, NTN, SC1N>(n, ring, 2);
  bar(); if constexpr (GN > 0) issue<GN, NTN, SC1N>(n, ring, 3);
}
DI int swz_wide(int k) { return 2 * ((k & 3) | (((k >> 3) & 1) << 2)); }
DI int swz_narrow(int k) { return (((k >> 1) & 1) << 2) | (((k >> 3) & 1) << 1); }
DI Src src_packed(i32x4 srd, int lane, unsigned base, unsigned kstep, int rot) {
  Src s; s.srd = srd; s.base = base; s.pstep = 4096; s.kstep = kstep; s.rot = rot;
  for (int g = 0; g < 4; ++g) s.voff[g] = (unsigned)lane * 16;
  return s;
}
DI Src src_rows(i32x4 srd, int lane, unsigned base, unsigned rowb, int rot) {
  Src s; s.srd = srd; s.base = base; s.pstep = 32 * rowb; s.kstep = 128; s.rot = rot;
  for (int g = 0; g < 4; ++g) { const int row = g * 8 + (lane >> 3), c = (lane & 7) ^ ((row >> 1) & 7); s.voff[g] = (unsigned)row * rowb + c * 16 - g * 1024; }
  return s;
}
DI Src src_narrow(i32x4 srd, int lane, unsigned colb, unsigned rowb, int rot) {
  Src s; s.srd = srd; s.base = colb; s.pstep = 32 * rowb; s.kstep = 64 * rowb; s.rot = rot;
  for (int g = 0; g < 4; ++g) { const int k = g * 8 + (lane >> 3), c = (lane & 7) ^ swz_narrow(k); s.voff[g] = (unsigned)k * rowb + c * 16 - g * 1024; }
  return s;
}

enum { B_WIDE = 0, B_NARROW = 1, B_NK = 2 };
template <int FM, int FN, int BMODE, int NT, int NORM_FROM>
DI void kloop(f32x4 (&acc)[FM][FN], const unsigned (&aaddr)[2], const unsigned (&bbase)[BMODE == B_NK ? 2 : FN]) {
  constexpr int NB = BMODE == B_NK ? 2 : FN;
  constexpr int RS = BMODE == B_WIDE ? 256 : 128;
  constexpr int RPH = FM + (BMODE == B_NK ? FN : 2 * FN), MPH = FM * FN;
  constexpr int RPM = (RPH + 3 > MPH) ? 2 : 1;
  s16x8 fa[2][FM], fb[2][FN]; s16x4 fbl[2][FN], fbh[2][FN];
  unsigned ca[2], cb[NB];
#pragma unroll
  for (int i = 0; i < FM; ++i)
#pragma unroll
    for (int j = 0; j < FN; ++j) acc[i][j] = (f32x4)(0.f);
  auto rd = [&](auto SET, auto KSI, auto R) {
    constexpr int set = SET.value, ks = KSI.value, rr = R.value, NBR = RPH - FM;
    if constexpr (rr == 0) fa[set][0] = lds_ld<s16x8>(ca[ks]);
    else if constexpr (rr <= NBR) {
      constexpr int x = rr - 1;
      if constexpr (BMODE == B_NK) fb[set][x] = lds_ld<s16x8>(cb[ks] + x * 2048);
      else { constexpr int j = x / 2, t = x % 2, o = (ks * 32 + t * 4) * RS;
        if constexpr (t == 0) fbl[set][j] = trread(cb[j] + o); else fbh[set][j] = trread(cb[j] + o); }
    } else { constexpr int i = rr - NBR; fa[set][i] = lds_ld<s16x8>(ca[ks] + i * 2048); }
  };
  auto mm = [&](auto SET, auto Q) {
    constexpr int set = SET.value, q = Q.value, i = q / FN, j = q % FN;
    s16x8 b; if constexpr (BMODE == B_NK) b = fb[set][j]; else b = __builtin_shufflevector(fbl[set][j], fbh[set][j], 0, 1, 2, 3, 4, 5, 6, 7);
    const s16x8 a = fa[set][i];
    if constexpr (j >= NORM_FROM) acc[i][j] = mfma16(a, b, acc[i][j]); else acc[i][j] = mfma16(b, a, acc[i][j]);
  };
  auto setbase = [&](unsigned so) {
#pragma unroll
    for (int ks = 0; ks < 2; ++ks) { ca[ks] = aaddr[ks] + so; asm volatile("" : "+v"(ca[ks])); }
#pragma unroll
    for (int j = 0; j < NB; ++j) { cb[j] = bbase[j] + so; asm volatile("" : "+v"(cb[j])); }
  };
  auto phase = [&](auto CS, auto NS, auto NKS) {
    static_for<0, MPH>([&](auto Q) {
      mm(CS, Q); SB();
      static_for<0, RPM>([&](auto U) { constexpr int r = Q.value * RPM + U.value; if constexpr (r < RPH) rd(NS, NKS, IC<r>{}); });
      SB();
    });
  };
  bar();
  setbase(0);
  static_for<0, RPH>([&](auto R) { rd(IC<0>{}, IC<0>{}, R); });
  for (int kt = 0; kt < NT; ++kt) {
    wait_lgkm0(); SB();
    phase(IC<0>{}, IC<1>{}, IC<1>{});
    setbase((unsigned)((kt + 1) & 3) * SLOT); SB();
    wait_lgkm0(); bar(); SB();
    phase(IC<1>{}, IC<0>{}, IC<0>{});
  }
  wait_lgkm0();
}
DI void a_addrs(unsigned (&aaddr)[2], unsigned base, int row0, int lane) {
#pragma unroll
  for (int ks = 0; ks < 2; ++ks) { const int row = row0 + (lane & 15), kc = ks * 4 + (lane >> 4); aaddr[ks] = base + row * 128 + ((kc ^ ((row >> 1) & 7)) * 16); }
}
DI unsigned tr_addr(unsigned base, int chunk0  , bool wide, int lane) {
  const int q = (lane & 15) >> 2, p = lane & 3, k = 8 * (lane >> 4) + q, c = chunk0 + (p >> 1);
  return wide ? base + k * 256 + ((c ^ swz_wide(k)) * 16) + (p & 1) * 8 : base + k * 128 + ((c ^ swz_narrow(k)) * 16) + (p & 1) * 8;
}
constexpr float L2E = 1.4426950408889634f;
DI void softmax_pm(f32x4 p0, f32x4 p1, f32x4& ep0, f32x4& ep1, f32x4& en0, f32x4& en1) {
  float mx = fmaxf(fmaxf(fmaxf(p0[0], p0[1]), fmaxf(p0[2], p0[3])), fmaxf(fmaxf(p1[0], p1[1]), fmaxf(p1[2], p1[3])));
  float mn = fminf(fminf(fminf(p0[0], p0[1]), fminf(p0[2], p0[3])), fminf(fminf(p1[0], p1[1]), fminf(p1[2], p1[3])));
  mx = x32_max(x16_max(mx)); mn = x32_min(x16_min(mn));
  const float a = -mx * L2E, b = mn * L2E;
  float sp = 0.f, sn = 0.f;
#pragma unroll
  for (int r = 0; r < 4; ++r) {
    ep0[r] = __builtin_amdgcn_exp2f(fmaf(p0[r], L2E, a)); ep1[r] = __builtin_amdgcn_exp2f(fmaf(p1[r], L2E, a));
    en0[r] = __builtin_amdgcn_exp2f(fmaf(p0[r], -L2E, b)); en1[r] = __builtin_amdgcn_exp2f(fmaf(p1[r], -L2E, b));
    sp += ep0[r] + ep1[r]; sn += en0[r] + en1[r];
  }
  sp = __builtin_amdgcn_rcpf(x32_add(x16_add(sp))); sn = __builtin_amdgcn_rcpf(x32_add(x16_add(sn)));
  ep0 *= sp; ep1 *= sp; en0 *= sn; en1 *= sn;
}
DI float gelu_t(float v) {
  const float k1 = -2.f * 0.7978845608028654f * L2E, k2 = k1 * 0.044715f;
  const float e = __builtin_amdgcn_exp2f(v * fmaf(v * v, k2, k1));
  return v * __builtin_amdgcn_rcpf(1.f + e);
}
DI s16x8 wphi_frag(const u16* wphi, int head, int ft, int lane) {
  const int f = 16 * ft + (lane & 15), g = lane >> 4; s16x8 r;
#pragma unroll
  for (int j = 0; j < 8; ++j) { const int d = (j < 4) ? 4 * g + j : 16 + 4 * g + j - 4; r[j] = (short)wphi[head * 1024 + d * 32 + f]; }
  return r;
}
template <int NJ> DI f32x2 row_stats(const f32x4 (&v)[NJ]) {
  float s = 0.f;
#pragma unroll
  for (int j = 0; j < NJ; ++j) s += (v[j][0] + v[j][1]) + (v[j][2] + v[j][3]);
  s = x32_add(x16_add(s));
  const float m = s * (1.f / (16 * NJ)); float q = 0.f;
#pragma unroll
  for (int j = 0; j < NJ; ++j) { const f32x4 d = v[j] - m; q += (d[0] * d[0] + d[1] * d[1]) + (d[2] * d[2] + d[3] * d[3]); }
  q = x32_add(x16_add(q));
  f32x2 r = {m, q}; return r;
}
template <int NP> DI f32x2 combine16(const u32x2 (&p)[16]) {
  float ms = 0.f;
#pragma unroll
  for (int t = 0; t < 16; ++t) ms += __uint_as_float(p[t][0]);
  const float mean = ms * (1.f / 16.f); float q = 0.f;
#pragma unroll
  for (int t = 0; t < 16; ++t) { const float dm = __uint_as_float(p[t][0]) - mean; q += __uint_as_float(p[t][1]) + (float)NP * dm * dm; }
  const float rstd = 1.0f / sqrtf(q * (1.f / (16 * NP)) + 1e-5f);
  f32x2 r = {rstd, -mean * rstd}; return r;
}

__global__ __launch_bounds__(512, 2) void fused_block(Params p) {
  extern __shared__ __attribute__((aligned(16))) char smem[];
  Ctx c;
  const int tid = threadIdx.x; c.lane = tid & 63; c.wid = __builtin_amdgcn_readfirstlane(tid >> 6);
  const int lane = c.lane, wid = c.wid;
  const int bid = blockIdx.x, xcd = bid & 7, idx = bid >> 3;
  constexpr int XM = 16 / GM, GN = 32 / GM;
  c.tm = (xcd % XM) * GM + idx % GM; c.tn = (xcd / XM) * GN + idx / GM;
  const int tm = c.tm, tn = c.tn;
  c.lds0 = (unsigned)(size_t)(LAS char*)smem; const unsigned lds0 = c.lds0;
  c.ws = mk_rsrc(p.ws, WS_END);
  c.t0 = now();
  const u32x2 ep = __builtin_amdgcn_raw_buffer_load_b64(c.ws, 0u, WS_EPOCH, 16);
  const int k0 = (xcd * 2 + (idx >> 4)) * 64 + (idx & 15) * 4;
  u32x2 wa0, wa1, wc0, wc1; float sc0, sc1; u32x4 xr0, xr1;
  {
    const u16* w0 = p.wmod + (size_t)tid * 6144 + k0; const u16* w1 = w0 + (size_t)512 * 6144;
    wa0 = *(const u32x2*)w0; wc0 = *(const u32x2*)(w0 + 1024); wa1 = *(const u32x2*)w1; wc1 = *(const u32x2*)(w1 + 1024);
    const float c0 = blo(p.c[tid]), c1 = blo(p.c[tid + 512]);
    const u16* xr = p.x + (size_t)(tm * 128 + tn * 8 + wid) * D + lane * 8;
    xr0 = *(const u32x4*)xr; xr1 = *(const u32x4*)(xr + 512);
    sc0 = bfr(c0 / (1.f + __expf(-c0))); sc1 = bfr(c1 / (1.f + __expf(-c1)));
  }
  {
    u64 e = ((u64)ep[1] << 32) | ep[0]; e += 0x9E3779B97F4A7C15ull; e = (e ^ (e >> 30)) * 0xBF58476D1CE4E5B9ull; e = (e ^ (e >> 27)) * 0x94D049BB133111EBull; e ^= e >> 31;
    c.tag[0] = __builtin_amdgcn_readfirstlane((unsigned)e); c.tag[1] = __builtin_amdgcn_readfirstlane((unsigned)(e >> 32));
  }
  float xv[16]; float ln_rs, ln_nb;
  {
    lds_st<float>(lds0 + G_SC + tid * 4, sc0); lds_st<float>(lds0 + G_SC + 2048 + tid * 4, sc1);
    float a[8];
    a[0] = sc0 * blo(wa0[0]) + sc1 * blo(wa1[0]); a[1] = sc0 * bhi(wa0[0]) + sc1 * bhi(wa1[0]); a[2] = sc0 * blo(wa0[1]) + sc1 * blo(wa1[1]); a[3] = sc0 * bhi(wa0[1]) + sc1 * bhi(wa1[1]);
    a[4] = sc0 * blo(wc0[0]) + sc1 * blo(wc1[0]); a[5] = sc0 * bhi(wc0[0]) + sc1 * bhi(wc1[0]); a[6] = sc0 * blo(wc0[1]) + sc1 * blo(wc1[1]); a[7] = sc0 * bhi(wc0[1]) + sc1 * bhi(wc1[1]);
#pragma unroll
    for (int i = 0; i < 8; ++i) a[i] = wave_sum(a[i]);
    if (lane == 0) { f32x4 v0 = {a[0], a[1], a[2], a[3]}, v1 = {a[4], a[5], a[6], a[7]}; lds_st<f32x4>(lds0 + G_RED + wid * 32, v0); lds_st<f32x4>(lds0 + G_RED + wid * 32 + 16, v1); }
    lbar();
    if (tid < 4) {
      float sa = 0.f, ca = 0.f;
#pragma unroll
      for (int w = 0; w < 8; ++w) { sa += lds_ld<float>(lds0 + G_RED + w * 32 + tid * 4); ca += lds_ld<float>(lds0 + G_RED + w * 32 + 16 + tid * 4); }
      u32x2 gr = {pk2(1.f + bfr(ca), sa), c.tag[0]};
      __builtin_amdgcn_raw_buffer_store_b64(gr, c.ws, (unsigned)(WS_G0 + (k0 + tid) * 8), 0, 16);
    }
#pragma unroll
    for (int i = 0; i < 4; ++i) { xv[2 * i] = blo(xr0[i]); xv[2 * i + 1] = bhi(xr0[i]); xv[8 + 2 * i] = blo(xr1[i]); xv[9 + 2 * i] = bhi(xr1[i]); }
    float s = 0.f;
#pragma unroll
    for (int i = 0; i < 16; ++i) s += xv[i];
    const float mean = wave_sum(s) * (1.f / D); float q = 0.f;
#pragma unroll
    for (int i = 0; i < 16; ++i) { const float d = xv[i] - mean; q += d * d; }
    ln_rs = 1.0f / sqrtf(wave_sum(q) * (1.f / D) + 1e-5f); ln_nb = -mean * ln_rs;
    for (;;) {
      const u32x4 g = __builtin_amdgcn_raw_buffer_load_b128(c.ws, (unsigned)(WS_G0 + tid * 16), 0, 16);
      const bool ok = __all(g[1] == c.tag[0] && g[3] == c.tag[0]);
      if (ok || now() - c.t0 > DEADLINE) { u32x2 v = {g[0], g[2]}; lds_st<u32x2>(lds0 + G_CS + tid * 8, v); break; }
      __builtin_amdgcn_s_sleep(1);
    }
    lbar();
    STAMP(1);
    const int row = tn * 8 + wid;
#pragma unroll
    for (int h = 0; h < 2; ++h) {
      const u32x4 g0 = lds_ld<u32x4>(lds0 + G_CS + h * 2048 + lane * 32), g1 = lds_ld<u32x4>(lds0 + G_CS + h * 2048 + lane * 32 + 16);
      float hv[8];
#pragma unroll
      for (int i = 0; i < 4; ++i) { hv[i] = fmaf(fmaf(xv[8 * h + i], ln_rs, ln_nb), blo(g0[i]), bhi(g0[i])); hv[4 + i] = fmaf(fmaf(xv[8 * h + 4 + i], ln_rs, ln_nb), blo(g1[i]), bhi(g1[i])); }
      u32x4 o = {pk2(hv[0], hv[1]), pk2(hv[2], hv[3]), pk2(hv[4], hv[5]), pk2(hv[6], hv[7])};
      const unsigned off = WS_HP + (unsigned)(tm * 16 + h * 8 + (lane >> 3)) * 16384 + row * 128 + (((lane & 7) ^ ((row >> 1) & 7)) * 16);
      __builtin_amdgcn_raw_buffer_store_b128(o, c.ws, off, 0, 16);
    }
    wait_vm<0>();
    set_flag(c, F_H, tm, row);
  }

  if (wid == 4) {
    const unsigned ring = lds0 + L_A;
    const i32x4 wsd = mk_srd(p.ws, WS_END);
    const Src sH = src_packed(wsd, lane, WS_HP + tm * 16 * 16384, 16384, tn);
    wait_flags(c, flag_off(F_H, tm, lane), true); wait_flags(c, flag_off(F_H, tm, 64 + lane), true);
    STAMP(2);
    issue4<16, 16, true>(sH, ring);
    load_pass<16, 16, true, 16, 16, true>(sH, sH, ring);
    bars<3>();
    load_pass<16, 16, true, 0, 16, true>(sH, sH, ring);
    bars<1>();
    const Src sO = src_packed(wsd, lane, WS_OP + tm * 16 * 16384, 16384, tn);
    wait_flags(c, flag_off(F_O, tm, lane), true);
    STAMP(3);
    issue4<16, 16, true>(sO, ring);
    load_pass<16, 16, true, 0, 16, true>(sO, sO, ring);
    const Src sX = src_packed(wsd, lane, WS_XP + tm * 16 * 16384, 16384, tn);
    wait_flags(c, flag_off(F_X, tm, lane), true);
    STAMP(4);
    issue4<16, 16, true>(sX, ring);
    load_pass<16, 16, true, 16, 16, true>(sX, sX, ring);
    bars<1>();
    const Src sW = src_rows(mk_srd(p.wsp, S * S * 2), lane, (unsigned)tm * 128 * 4096, 4096, 2 * tm);
    load_pass<16, 16, true, 16, 32, false>(sX, sW, ring);
    load_pass<16, 32, false, 0, 32, false>(sW, sW, ring);
    const Src sY = src_packed(wsd, lane, WS_YP + tm * 28 * 16384, 16384, (tn * 7) >> 2);
    wait_flags(c, flag_off(F_Y, tm, lane), true);
    STAMP(5);
    issue4<16, 28, true>(sY, ring);
    load_pass<16, 28, true, 0, 28, true>(sY, sY, ring);
    return;
  }
  if (wid == 5) {
    const unsigned ring = lds0 + L_B;
    Src sB0;
    sB0.srd = mk_srd(p.wqkv, D * 3 * D * 2); sB0.base = 0; sB0.pstep = 16 * 6144; sB0.kstep = 64 * 6144; sB0.rot = tn;
    for (int g = 0; g < 4; ++g) { const int k = g * 4 + (lane >> 4), cc = (lane & 15) ^ swz_wide(k); const int col = 1024 + (cc >> 3) * 1024 + 64 * tn + 8 * (cc & 7); sB0.voff[g] = (unsigned)(k * 6144 + col * 2 - g * 1024); }
    issue4<16, 16, false>(sB0, ring);
    const Src sQ = src_narrow(sB0.srd, lane, 128 * tn, 6144, tn);
    load_pass<16, 16, false, 8, 16, false>(sB0, sQ, ring);
    bars<3>();
    const Src sWo = src_narrow(mk_srd(p.wo, D * D * 2), lane, 128 * tn, 2048, tn);
    load_pass<8, 16, false, 8, 16, false>(sQ, sWo, ring);
    bars<1>();
    Src sZ2; sZ2.srd = mk_srd(p.win, D * FF * 2); sZ2.base = FH * 2; sZ2.pstep = 16 * 7168; sZ2.kstep = 64 * 7168; sZ2.rot = tn;
    for (int g = 0; g < 4; ++g) { const int k = g * 4 + (lane >> 4), cc = ((lane & 15) ^ swz_wide(k)) - 2 * (tn & 1);
      sZ2.voff[g] = (cc >= 0 && cc < 14) ? (unsigned)(k * 7168 + (112 * tn + 8 * cc) * 2 - g * 1024) : 0x80000000u; }
    Src sZ1 = sZ2; sZ1.base = 0;
    load_pass<8, 16, false, 16, 16, false>(sWo, sZ2, ring);
    load_pass<16, 16, false, 16, 16, false>(sZ2, sZ1, ring);
    bars<1>();
    load_pass<16, 16, false, 0, 16, false>(sZ1, sZ1, ring);
    const Src sZ = src_packed(mk_srd(p.ws, WS_END), lane, WS_ZP + tn * 32 * 14336, 14336, 2 * tm);
    wait_flags(c, flag_off(F_Z, tn, lane & 31), true);
    issue4<14, 32, true>(sZ, ring);
    const Src sWout = src_narrow(mk_srd(p.wout, FH * D * 2), lane, 128 * tn, 2048, (tn * 7) >> 2);
    load_pass<14, 32, true, 8, 28, false>(sZ, sWout, ring);
    load_pass<8, 28, false, 0, 28, false>(sWout, sWout, ring);
    return;
  }
  if (wid >= 6) {
    const int a = wid - 6, u = tm * 16 + tn;
    {
      const int cbk = u >> 2, r0 = (u & 3) * 256 + a * 128;
      const u16* w = p.wmod + (size_t)(r0 + (lane >> 3)) * 6144 + 2048 + cbk * 64 + (lane & 7) * 8;
      u32x4 wv[16];
#pragma unroll
      for (int i = 0; i < 16; ++i) wv[i] = *(const u32x4*)(w + (size_t)i * 8 * 6144);
      bars<5>();
      float acc[8];
#pragma unroll
      for (int e = 0; e < 8; ++e) acc[e] = 0.f;
#pragma unroll
      for (int i = 0; i < 16; ++i) {
        const float s = lds_ld<float>(lds0 + G_SC + (r0 + (lane >> 3) + 8 * i) * 4);
#pragma unroll
        for (int e = 0; e < 4; ++e) { acc[2 * e] = fmaf(s, blo(wv[i][e]), acc[2 * e]); acc[2 * e + 1] = fmaf(s, bhi(wv[i][e]), acc[2 * e + 1]); }
        if ((i & 3) == 3) { asm volatile("" : "+v"(acc[0]), "+v"(acc[1]), "+v"(acc[2]), "+v"(acc[3]), "+v"(acc[4]), "+v"(acc[5]), "+v"(acc[6]), "+v"(acc[7])); bar(); }
      }
#pragma unroll
      for (int e = 0; e < 8; ++e) acc[e] = x32_add(x16_add(dpp_add<0x128>(acc[e])));
      if (lane < 8) {
        const unsigned off = WS_MP + (unsigned)((cbk * 8 + (u & 3) * 2 + a) * 64 + lane * 8) * 4;
        u32x4 v0 = {__float_as_uint(acc[0]), __float_as_uint(acc[1]), __float_as_uint(acc[2]), __float_as_uint(acc[3])};
        u32x4 v1 = {__float_as_uint(acc[4]), __float_as_uint(acc[5]), __float_as_uint(acc[6]), __float_as_uint(acc[7])};
        __builtin_amdgcn_raw_buffer_store_b128(v0, c.ws, off, 0, 16); __builtin_amdgcn_raw_buffer_store_b128(v1, c.ws, off + 16, 0, 16);
      }
      bars<3>();
      wait_vm<0>(); set_flag(c, F_M, 0, cbk * 8 + (u & 3) * 2 + a);
      bars<5>();
    }
    bars<3>();
    {
      const int t = a * 64 + lane;
#pragma unroll
      for (int i = 0; i < 9; ++i) { const int q = t + 128 * i; if (q < 1056) __builtin_amdgcn_raw_buffer_store_b128(lds_ld<u32x4>(lds0 + L_STG + q * 16), c.ws, WS_KVP + (unsigned)(tn * 16 + tm) * KVB + q * 16, 0, 16); }
      bars<3>();
      wait_vm<0>(); set_flag(c, F_KV, tn, tm * 2 + a);
      bars<4>();
      wait_flags(c, flag_off(F_KV, tn, lane & 31), true);
      const unsigned so = (unsigned)(tm * 66 + a * 33 + (lane < 33 ? lane : 0)) * 16;
      u32x4 pv[16];
#pragma unroll
      for (int i = 0; i < 16; ++i) pv[i] = __builtin_amdgcn_raw_buffer_load_b128(c.ws, WS_KVP + (unsigned)(tn * 16 + i) * KVB + so, 0, 16);
      bars<2>();
      f32x4 sum = __builtin_bit_cast(f32x4, pv[0]);
#pragma unroll
      for (int i = 1; i < 16; ++i) sum += __builtin_bit_cast(f32x4, pv[i]);
      if (lane < 33) __builtin_amdgcn_raw_buffer_store_b128(__builtin_bit_cast(u32x4, sum), c.ws, WS_KVS + (unsigned)tn * KVB + so, 0, 16);
      bars<2>();
      wait_vm<0>(); set_flag(c, F_KS, tn, tm * 2 + a);
      bars<4>();
      wait_flags(c, flag_off(F_KS, tn, lane & 31), true);
      u32x4 kvv[9];
#pragma unroll
      for (int i = 0; i < 9; ++i) { const int q = t + 128 * i; kvv[i] = __builtin_amdgcn_raw_buffer_load_b128(c.ws, WS_KVS + (unsigned)tn * KVB + (q < 1056 ? q : 0) * 16, 0, 16); }
      bars<2>();
#pragma unroll
      for (int i = 0; i < 9; ++i) {
        const int q = t + 128 * i, hh = q >= 528 ? 1 : 0, qq = q - hh * 528;
        unsigned dst;
        if (qq < 512) { const int tile = qq >> 6, ft = tile >> 1, dt = tile & 1; dst = ((dt * 2 + (ft >> 1)) * 64 + (qq & 63)) * 16 + (ft & 1) * 8; }
        else { const int ft = (qq - 512) >> 2, g = qq & 3; dst = 4096 + ((ft >> 1) * 4 + g) * 16 + (ft & 1) * 8; }
        if (q < 1056) lds_st<u32x2>(lds0 + G_KVF + hh * 4224 + dst, pk4(__builtin_bit_cast(f32x4, kvv[i])));
      }
      lbar();
    }
    {
      const int ch = lane & 15, rg = lane >> 4;
      const u16* w = p.win + (size_t)(64 * tm + rg) * FF + (a ? 0 : FH) + 112 * tn + (ch < 14 ? ch : 0) * 8;
      u32x4 wv[16];
#pragma unroll
      for (int i = 0; i < 16; ++i) wv[i] = *(const u32x4*)(w + (size_t)i * 4 * FF);
      const int v5 = lane >> 3; const int blk = v5 == 0 ? tn : v5 == 1 ? 32 + tn : v5 == 2 ? 48 + tn : v5 == 3 ? 32 + tm : 16 + tm;
      wait_flags(c, flag_off(F_M, 0, blk * 8 + (lane & 7)), lane < 40);
      auto vec = [&](int b) { float s = 0.f;
#pragma unroll
        for (int i = 0; i < 8; ++i) s += __uint_as_float(__builtin_amdgcn_raw_buffer_load_b32(c.ws, WS_MP + (unsigned)((b * 8 + i) * 64 + lane) * 4, 0, 16));
        return bfr(s); };
      if (a == 0) {
        const float ga = vec(tn), cb = vec(32 + tn), gb = vec(48 + tn);
        lds_st<float>(lds0 + T_X + lane * 4, ga); lds_st<float>(lds0 + T_X + 256 + lane * 4, bfr(1.f + cb)); lds_st<float>(lds0 + T_GB + lane * 4, gb);
      } else {
        const float cb = vec(32 + tm), sb = vec(16 + tm);
        f32x2 v = {bfr(1.f + cb), sb}; lds_st<f32x2>(lds0 + G_KT + lane * 8, v);
      }
      lbar();
      float v1[8], v2[8];
#pragma unroll
      for (int e = 0; e < 8; ++e) { v1[e] = 0.f; v2[e] = 0.f; }
#pragma unroll
      for (int i = 0; i < 16; ++i) {
        const f32x2 s = lds_ld<f32x2>(lds0 + G_KT + (rg + 4 * i) * 8);
#pragma unroll
        for (int e = 0; e < 4; ++e) { const float l = blo(wv[i][e]), h = bhi(wv[i][e]);
          v1[2 * e] = fmaf(s[0], l, v1[2 * e]); v1[2 * e + 1] = fmaf(s[0], h, v1[2 * e + 1]); v2[2 * e] = fmaf(s[1], l, v2[2 * e]); v2[2 * e + 1] = fmaf(s[1], h, v2[2 * e + 1]); }
        if (i & 1) { asm volatile("" : "+v"(v1[0]), "+v"(v1[1]), "+v"(v1[2]), "+v"(v1[3]), "+v"(v1[4]), "+v"(v1[5]), "+v"(v1[6]), "+v"(v1[7]));
                     asm volatile("" : "+v"(v2[0]), "+v"(v2[1]), "+v"(v2[2]), "+v"(v2[3]), "+v"(v2[4]), "+v"(v2[5]), "+v"(v2[6]), "+v"(v2[7])); bar(); }
      }
#pragma unroll
      for (int e = 0; e < 8; ++e) { v1[e] = x32_add(x16_add(v1[e])); v2[e] = x32_add(x16_add(v2[e])); }
      if (lane < 14) {
        const unsigned off = WS_VP + (unsigned)((tn * 16 + tm) * 448 + a * 112 + lane * 8) * 4;
        u32x4 q0 = {__float_as_uint(v1[0]), __float_as_uint(v1[1]), __float_as_uint(v1[2]), __float_as_uint(v1[3])}, q1 = {__float_as_uint(v1[4]), __float_as_uint(v1[5]), __float_as_uint(v1[6]), __float_as_uint(v1[7])};
        u32x4 q2 = {__float_as_uint(v2[0]), __float_as_uint(v2[1]), __float_as_uint(v2[2]), __float_as_uint(v2[3])}, q3 = {__float_as_uint(v2[4]), __float_as_uint(v2[5]), __float_as_uint(v2[6]), __float_as_uint(v2[7])};
        __builtin_amdgcn_raw_buffer_store_b128(q0, c.ws, off, 0, 16); __builtin_amdgcn_raw_buffer_store_b128(q1, c.ws, off + 16, 0, 16);
        __builtin_amdgcn_raw_buffer_store_b128(q2, c.ws, off + 896, 0, 16); __builtin_amdgcn_raw_buffer_store_b128(q3, c.ws, off + 912, 0, 16);
      }
      bars<3>();
      wait_vm<0>(); set_flag(c, F_V, tn, tm * 2 + a);
      bars<5>();
    }
    {
      wait_flags(c, flag_off(F_V, tn, lane & 31), true);
      u32x4 pv[16];
      const int q = a * 56 + (lane < 56 ? lane : 0);
#pragma unroll
      for (int i = 0; i < 16; ++i) pv[i] = __builtin_amdgcn_raw_buffer_load_b128(c.ws, WS_VP + (unsigned)((tn * 16 + i) * 448) * 4 + q * 16, 0, 16);
      f32x4 sum = __builtin_bit_cast(f32x4, pv[0]);
#pragma unroll
      for (int i = 1; i < 16; ++i) sum += __builtin_bit_cast(f32x4, pv[i]);
      if (lane < 56) lds_st<f32x4>(lds0 + T_V1 + q * 16, sum);
      wait_flags(c, flag_off(F_X, tm, lane), true);
      u32x2 ps[16];
#pragma unroll
      for (int i = 0; i < 16; ++i) ps[i] = __builtin_amdgcn_raw_buffer_load_b64(c.ws, WS_XS + (unsigned)((tm * 16 + i) * 128 + a * 64 + lane) * 8, 0, 16);
      bars<4>();
      lds_st<f32x2>(lds0 + T_RS + (a * 64 + lane) * 8, combine16<64>(ps));
      lbar();
      bars<12>();
    }
    bars<1>();
    {
      __builtin_amdgcn_raw_buffer_store_b64(lds_ld<u32x2>(lds0 + T_X + (a * 64 + lane) * 8), c.ws, WS_ZS + (unsigned)((tm * 16 + tn) * 128 + a * 64 + lane) * 8, 0, 16);
      bars<3>();
      wait_vm<0>(); set_flag(c, F_ZS, tm, tn * 2 + a);
      bars<3>();
      wait_flags(c, flag_off(F_ZS, tm, lane & 31), true);
      u32x2 ps[16];
#pragma unroll
      for (int i = 0; i < 16; ++i) ps[i] = __builtin_amdgcn_raw_buffer_load_b64(c.ws, WS_ZS + (unsigned)((tm * 16 + i) * 128 + a * 64 + lane) * 8, 0, 16);
      bars<2>();
      lds_st<f32x2>(lds0 + T_X + (a * 64 + lane) * 8, combine16<112>(ps));
      wait_lgkm0();
      const int g = lane >> 4, i16 = lane & 15, q = i16 >> 2, pp = i16 & 3;
#pragma unroll
      for (int cq = 0; cq < 2; ++cq) {
        const int ch = cq * 4 + g, s0 = a * 64 + ch * 8;
        f32x4 sv[4];
#pragma unroll
        for (int e = 0; e < 4; ++e) sv[e] = lds_ld<f32x4>(lds0 + T_X + (s0 + 2 * e) * 8);
#pragma unroll
        for (int ft = 0; ft < 7; ++ft) {
          const unsigned ra = lds0 + L_STG + (s0 + q) * 224 + ft * 32 + pp * 8;
          const u32x2 lo = __builtin_bit_cast(u32x2, trread(ra)), hi = __builtin_bit_cast(u32x2, trread(ra + 4 * 224));
          u32x4 o;
          o[0] = pk2(fmaf(blo(lo[0]), sv[0][0], sv[0][1]), fmaf(bhi(lo[0]), sv[0][2], sv[0][3]));
          o[1] = pk2(fmaf(blo(lo[1]), sv[1][0], sv[1][1]), fmaf(bhi(lo[1]), sv[1][2], sv[1][3]));
          o[2] = pk2(fmaf(blo(hi[0]), sv[2][0], sv[2][1]), fmaf(bhi(hi[0]), sv[2][2], sv[2][3]));
          o[3] = pk2(fmaf(blo(hi[1]), sv[3][0], sv[3][1]), fmaf(bhi(hi[1]), sv[3][2], sv[3][3]));
          const int f = ft * 16 + i16;
          __builtin_amdgcn_raw_buffer_store_b128(o, c.ws, WS_ZP + (unsigned)(tn * 32 + tm * 2 + a) * 14336 + f * 128 + ((ch ^ ((f >> 1) & 7)) * 16), 0, 16);
          if (ft == 3 || ft == 6) bar();
        }
      }
      bars<2>();
      wait_vm<0>(); set_flag(c, F_Z, tn, tm * 2 + a);
      bars<3>();
    }
    {
      const float b = blo(p.bsp[tm * 128 + a * 64 + lane]);
      bars<2>();
      lds_st<float>(lds0 + T_X + (a * 64 + lane) * 4, b);
      lbar();
      bars<30>();
    }
    bars<29>();
    if (u == 0 && a == 0 && lane == 0) {
      const u64 e = (((u64)ep[1] << 32) | ep[0]) + 1; u32x2 v = {(unsigned)e, (unsigned)(e >> 32)};
      __builtin_amdgcn_raw_buffer_store_b64(v, c.ws, 0u, WS_EPOCH, 16);
    }
    return;
  }

  const int g = lane >> 4, i16 = lane & 15;
  unsigned aaddr[2];
  {
    const int wm = wid >> 1, wn = wid & 1;
    s16x8 wph[2];
    wph[0] = wphi_frag(p.wphi, 2 * tn + wn, 0, lane); wph[1] = wphi_frag(p.wphi, 2 * tn + wn, 1, lane);
    unsigned bb[4];
    a_addrs(aaddr, lds0 + L_A, 64 * wm, lane);
#pragma unroll
    for (int j = 0; j < 4; ++j) bb[j] = tr_addr(lds0 + L_B, (j >> 1) * 8 + 4 * wn + 2 * (j & 1), true, lane);
    f32x4 acc[4][4];
    kloop<4, 4, B_WIDE, 16, 2>(acc, aaddr, bb);
    STAMP(2);
    f32x4 kv[4][2], zz[4];
#pragma unroll
    for (int ft = 0; ft < 4; ++ft) { kv[ft][0] = (f32x4)(0.f); kv[ft][1] = (f32x4)(0.f); zz[ft] = (f32x4)(0.f); }
    const s16x8 ones = {0x3f80, 0x3f80, 0x3f80, 0x3f80, 0x3f80, 0x3f80, 0x3f80, 0x3f80};
    const unsigned tb = lds0 + L_STG + wid * 4096;
#pragma unroll
    for (int hb = 0; hb < 2; ++hb) {
#pragma unroll
      for (int ii = 0; ii < 2; ++ii) {
        const s16x8 kf = pk8(acc[2 * hb + ii][0], acc[2 * hb + ii][1]);
        const f32x4 p0 = mfma16(wph[0], kf, (f32x4)(0.f)), p1 = mfma16(wph[1], kf, (f32x4)(0.f));
        f32x4 e[4]; softmax_pm(p0, p1, e[0], e[1], e[2], e[3]);
        const int row = ii * 16 + i16;
#pragma unroll
        for (int ft = 0; ft < 4; ++ft) lds_st<u32x2>(tb + row * 128 + ((ft ^ ((row >> 1) & 3)) * 32) + g * 8, pk4(e[ft]));
      }
      wait_lgkm0();
      const s16x8 v0 = pk8(acc[2 * hb][2], acc[2 * hb + 1][2]), v1 = pk8(acc[2 * hb][3], acc[2 * hb + 1][3]);
      const int rr = 4 * g + (i16 >> 2);
#pragma unroll
      for (int ft = 0; ft < 4; ++ft) {
        const unsigned ra = tb + rr * 128 + ((ft ^ ((rr >> 1) & 3)) * 32) + (i16 & 3) * 8;
        const s16x4 lo = trread(ra), hi = trread(ra + 2048);
        const s16x8 pf = __builtin_shufflevector(lo, hi, 0, 1, 2, 3, 4, 5, 6, 7);
        kv[ft][0] = mfma16(pf, v0, kv[ft][0]); kv[ft][1] = mfma16(pf, v1, kv[ft][1]); zz[ft] = mfma16(pf, ones, zz[ft]);
      }
      wait_lgkm0();
    }
    const unsigned pb = lds0 + L_STG + wn * 8448;
    lbar();
    if (wm == 1) {
#pragma unroll
      for (int ft = 0; ft < 4; ++ft) { lds_st<f32x4>(pb + ((ft * 2) * 64 + lane) * 16, kv[ft][0]); lds_st<f32x4>(pb + ((ft * 2 + 1) * 64 + lane) * 16, kv[ft][1]); if (i16 == 0) lds_st<f32x4>(pb + 8192 + (ft * 4 + g) * 16, zz[ft]); }
    }
    lbar();
    if (wm == 0) {
#pragma unroll
      for (int ft = 0; ft < 4; ++ft) {
        const unsigned a0 = pb + ((ft * 2) * 64 + lane) * 16, a1 = a0 + 1024, a2 = pb + 8192 + (ft * 4 + g) * 16;
        lds_st<f32x4>(a0, lds_ld<f32x4>(a0) + kv[ft][0]); lds_st<f32x4>(a1, lds_ld<f32x4>(a1) + kv[ft][1]); if (i16 == 0) lds_st<f32x4>(a2, lds_ld<f32x4>(a2) + zz[ft]);
      }
    }
    lbar();
  }
  STAMP(3);
  a_addrs(aaddr, lds0 + L_A, 32 * wid, lane);
  unsigned bn[4];
#pragma unroll
  for (int j = 0; j < 4; ++j) bn[j] = tr_addr(lds0 + L_B, 2 * j, false, lane);
  const unsigned sg = lds0 + L_STG + wid * 4096;
  auto put_rows = [&](unsigned dst) {
    wait_lgkm0();
    u32x4 v[4];
#pragma unroll
    for (int i = 0; i < 4; ++i) v[i] = lds_ld<u32x4>(sg + i * 1024 + lane * 16);
#pragma unroll
    for (int i = 0; i < 4; ++i) __builtin_amdgcn_raw_buffer_store_b128(v[i], c.ws, dst + wid * 4096 + i * 1024 + lane * 16, 0, 16);
  };
  auto img = [&](int i, int j) { const int row = 16 * i + i16; return sg + row * 128 + (((2 * j + (g >> 1)) ^ ((row >> 1) & 7)) * 16) + (g & 1) * 8; };
  {
    s16x8 wph[2][2];
#pragma unroll
    for (int hh = 0; hh < 2; ++hh) { wph[hh][0] = wphi_frag(p.wphi, 2 * tn + hh, 0, lane); wph[hh][1] = wphi_frag(p.wphi, 2 * tn + hh, 1, lane); }
    f32x4 acc[2][4];
    kloop<2, 4, B_NARROW, 16, 99>(acc, aaddr, bn);
    STAMP(4);
    s16x8 fp[2][2], fn[2][2];
#pragma unroll
    for (int i = 0; i < 2; ++i)
#pragma unroll
      for (int hh = 0; hh < 2; ++hh) {
        const s16x8 qf = pk8(acc[i][2 * hh], acc[i][2 * hh + 1]);
        const f32x4 p0 = mfma16(wph[hh][0], qf, (f32x4)(0.f)), p1 = mfma16(wph[hh][1], qf, (f32x4)(0.f));
        f32x4 e0, e1, e2, e3; softmax_pm(p0, p1, e0, e1, e2, e3);
        fp[i][hh] = pk8(e0, e1); fn[i][hh] = pk8(e2, e3);
      }
    bar();
    STAMP(5);
#pragma unroll
    for (int hh = 0; hh < 2; ++hh) {
      const unsigned kb = lds0 + G_KVF + hh * 4224;
      const s16x8 k00 = lds_ld<s16x8>(kb + lane * 16), k01 = lds_ld<s16x8>(kb + 1024 + lane * 16), k10 = lds_ld<s16x8>(kb + 2048 + lane * 16), k11 = lds_ld<s16x8>(kb + 3072 + lane * 16);
      const s16x8 z0 = lds_ld<s16x8>(kb + 4096 + g * 16), z1 = lds_ld<s16x8>(kb + 4096 + 64 + g * 16);
#pragma unroll
      for (int i = 0; i < 2; ++i) {
        f32x4 n0 = mfma16(k01, fn[i][hh], mfma16(k00, fp[i][hh], (f32x4)(0.f)));
        f32x4 n1 = mfma16(k11, fn[i][hh], mfma16(k10, fp[i][hh], (f32x4)(0.f)));
        const f32x4 dn = mfma16(z1, fn[i][hh], mfma16(z0, fp[i][hh], (f32x4)(0.f)));
        const float r = __builtin_amdgcn_rcpf(dn[0] + 1e-6f);
        lds_st<u32x2>(img(i, 2 * hh), pk4(n0 * r)); lds_st<u32x2>(img(i, 2 * hh + 1), pk4(n1 * r));
      }
    }
    put_rows(WS_OP + (unsigned)(tm * 16 + tn) * 16384);
    wait_vm<0>(); set_flag(c, F_O, tm, tn * 4 + wid);
  }
  STAMP(6);
  u32x2 x1p[2][4];
  {
    u32x2 xin[2][4];
#pragma unroll
    for (int i = 0; i < 2; ++i)
#pragma unroll
      for (int j = 0; j < 4; ++j) xin[i][j] = *(const u32x2*)(p.x + (size_t)(tm * 128 + 32 * wid + 16 * i + i16) * D + 64 * tn + 16 * j + 4 * g);
    f32x4 acc[2][4];
    kloop<2, 4, B_NARROW, 16, 99>(acc, aaddr, bn);
    STAMP(7);
#pragma unroll
    for (int i = 0; i < 2; ++i) {
      f32x4 x1[4];
#pragma unroll
      for (int j = 0; j < 4; ++j) {
        const f32x4 ga = lds_ld<f32x4>(lds0 + T_X + (16 * j + 4 * g) * 4), cb = lds_ld<f32x4>(lds0 + T_X + 256 + (16 * j + 4 * g) * 4);
        const f32x4 xv4 = {blo(xin[i][j][0]), bhi(xin[i][j][0]), blo(xin[i][j][1]), bhi(xin[i][j][1])};
        x1p[i][j] = pk4(xv4 + ga * acc[i][j]);
        x1[j] = (f32x4){blo(x1p[i][j][0]), bhi(x1p[i][j][0]), blo(x1p[i][j][1]), bhi(x1p[i][j][1])};
        lds_st<u32x2>(img(i, j), pk4(x1[j] * cb));
      }
      const f32x2 stt = row_stats<4>(x1);
      if (g == 0) __builtin_amdgcn_raw_buffer_store_b64(__builtin_bit_cast(u32x2, stt), c.ws, WS_XS + (unsigned)((tm * 16 + tn) * 128 + 32 * wid + 16 * i + i16) * 8, 0, 16);
    }
#pragma unroll
    for (int i = 0; i < 2; ++i)
#pragma unroll
      for (int j = 0; j < 4; ++j) asm volatile("" : "+v"(x1p[i][j]));
    put_rows(WS_XP + (unsigned)(tm * 16 + tn) * 16384);
    wait_vm<0>(); set_flag(c, F_X, tm, tn * 4 + wid);
  }
  STAMP(8);
  u32x2 z1p[2][7];
  {
    unsigned bw[7];
#pragma unroll
    for (int j = 0; j < 7; ++j) bw[j] = tr_addr(lds0 + L_B, 2 * j + 2 * (tn & 1), true, lane);
    auto act = [&](f32x4 (&acc)[2][7], int half) {
#pragma unroll
      for (int i = 0; i < 2; ++i) {
        const f32x2 rs = lds_ld<f32x2>(lds0 + T_RS + (32 * wid + 16 * i + i16) * 8);
#pragma unroll
        for (int j = 0; j < 7; ++j) {
          const f32x4 v1 = lds_ld<f32x4>(lds0 + T_V1 + (half * 112 + 16 * j + 4 * g) * 4), v2 = lds_ld<f32x4>(lds0 + T_V2 + (half * 112 + 16 * j + 4 * g) * 4);
#pragma unroll
          for (int r = 0; r < 4; ++r) acc[i][j][r] = gelu_t(fmaf(acc[i][j][r], rs[0], fmaf(rs[1], v1[r], v2[r])));
        }
      }
    };
    {
      f32x4 acc[2][7];
      kloop<2, 7, B_WIDE, 16, 99>(acc, aaddr, bw);
      STAMP(9);
      act(acc, 0);
#pragma unroll
      for (int i = 0; i < 2; ++i) {
        const int row = 32 * wid + 16 * i + i16;
#pragma unroll
        for (int j = 0; j < 7; ++j) {
          const u32x2 w = pk4(acc[i][j]);
          lds_st<u32x2>(lds0 + L_STG + row * 224 + j * 32 + g * 8, w);
          acc[i][j] = (f32x4){blo(w[0]), bhi(w[0]), blo(w[1]), bhi(w[1])};
        }
        const f32x2 stt = row_stats<7>(acc[i]);
        if (g == 0) lds_st<f32x2>(lds0 + T_X + row * 8, stt);
      }
      lbar();
    }
    STAMP(10);
    {
      f32x4 acc[2][7];
      kloop<2, 7, B_WIDE, 16, 99>(acc, aaddr, bw);
      STAMP(11);
      act(acc, 1);
#pragma unroll
      for (int i = 0; i < 2; ++i)
#pragma unroll
        for (int j = 0; j < 7; ++j) { z1p[i][j] = pk4(acc[i][j]); asm volatile("" : "+v"(z1p[i][j])); }
    }
  }
  STAMP(12);
  {
    unsigned bk[2];
    a_addrs(bk, lds0 + L_B, 0, lane);
    f32x4 acc[2][7];
    kloop<2, 7, B_NK, 32, 99>(acc, aaddr, bk);
    STAMP(13);
    const unsigned sy = lds0 + L_STG + wid * 7168;
#pragma unroll
    for (int i = 0; i < 2; ++i) {
      const float b = lds_ld<float>(lds0 + T_X + (32 * wid + 16 * i + i16) * 4);
#pragma unroll
      for (int j = 0; j < 7; ++j) {
        const f32x4 z = {blo(z1p[i][j][0]), bhi(z1p[i][j][0]), blo(z1p[i][j][1]), bhi(z1p[i][j][1])};
        lds_st<u32x2>(sy + (16 * i + i16) * 224 + j * 32 + g * 8, pk4(z * (acc[i][j] + b)));
      }
    }
    wait_lgkm0();
    u32x4 v[7];
#pragma unroll
    for (int i = 0; i < 7; ++i) v[i] = lds_ld<u32x4>(sy + i * 1024 + lane * 16);
#pragma unroll
    for (int i = 0; i < 7; ++i) {
      const int n = i * 64 + lane, r = (n * 4682) >> 16  , ch = n - r * 14, row = 32 * wid + r, gc = 14 * tn + ch;
      __builtin_amdgcn_raw_buffer_store_b128(v[i], c.ws, WS_YP + (unsigned)(tm * 28 + (gc >> 3)) * 16384 + row * 128 + (((gc & 7) ^ ((row >> 1) & 7)) * 16), 0, 16);
    }
    wait_vm<0>(); set_flag(c, F_Y, tm, tn * 4 + wid);
  }
  STAMP(14);
  {
    f32x4 acc[2][4];
    kloop<2, 4, B_NARROW, 28, 99>(acc, aaddr, bn);
#pragma unroll
    for (int i = 0; i < 2; ++i)
#pragma unroll
      for (int j = 0; j < 4; ++j) {
        const f32x4 gb = lds_ld<f32x4>(lds0 + T_GB + (16 * j + 4 * g) * 4);
        const f32x4 x1 = {blo(x1p[i][j][0]), bhi(x1p[i][j][0]), blo(x1p[i][j][1]), bhi(x1p[i][j][1])};
        lds_st<u32x2>(img(i, j), pk4(x1 + gb * acc[i][j]));
      }
    wait_lgkm0();
#pragma unroll
    for (int i = 0; i < 4; ++i) {
      const int row = i * 8 + (lane >> 3);
      const u32x4 v = lds_ld<u32x4>(sg + row * 128 + (((lane & 7) ^ ((row >> 1) & 7)) * 16));
      *(u32x4*)(p.out + (size_t)(tm * 128 + 32 * wid + row) * D + 64 * tn + (lane & 7) * 8) = v;
    }
  }
}

extern "C" void kernel_launch(void* const* d_in, const int* in_sizes, int n_in, void* d_out, int out_size, void* d_ws, size_t ws_size, hipStream_t stream) {
  Params p;
  p.x = (const u16*)d_in[0]; p.wqkv = (const u16*)d_in[1]; p.wphi = (const u16*)d_in[2]; p.wo = (const u16*)d_in[3];
  p.win = (const u16*)d_in[5]; p.wsp = (const u16*)d_in[6]; p.bsp = (const u16*)d_in[7]; p.wout = (const u16*)d_in[8];
  p.c = (const u16*)d_in[9]; p.wmod = (const u16*)d_in[10]; p.out = (u16*)d_out; p.ws = (char*)d_ws;
  (void)hipFuncSetAttribute((const void*)fused_block, hipFuncAttributeMaxDynamicSharedMemorySize, L_TOTAL);
  hipLaunchKernelGGL(fused_block, dim3(256), dim3(512), L_TOTAL, stream, p);
}
```

```cpp
#define V_WQ 0
#define V_B0 1
#define V_ORD 0
#include <hip/hip_runtime.h>
#include <stdint.h>
#include <type_traits>

typedef unsigned short u16;
typedef unsigned long long u64;
typedef short s16x4 __attribute__((ext_vector_type(4)));
typedef short s16x8 __attribute__((ext_vector_type(8)));
typedef __bf16 bf16x8 __attribute__((ext_vector_type(8)));
typedef __bf16 bf16x2 __attribute__((ext_vector_type(2)));
typedef float f32x2 __attribute__((ext_vector_type(2)));
typedef float f32x4 __attribute__((ext_vector_type(4)));
typedef unsigned u32x2 __attribute__((ext_vector_type(2)));
typedef unsigned u32x4 __attribute__((ext_vector_type(4)));
typedef int i32x4 __attribute__((ext_vector_type(4)));
typedef __amdgpu_buffer_rsrc_t rsrc_t;
#define LAS __attribute__((address_space(3)))
#define DI __device__ __forceinline__
#define SB() __builtin_amdgcn_sched_barrier(0)
template <int V> using IC = std::integral_constant<int, V>;
template <int I, int N, class Fn> DI void static_for(Fn&& f) { if constexpr (I < N) { f(IC<I>{}); static_for<I + 1, N>(f); } }

constexpr int S = 2048, D = 1024, FF = 3584, FH = 1792;
#define V_DLY 0
#define V_CAD 4
#define V_FSTR 8
#define V_S0 2
#define V_S1 20
#define V_S2 4
#define V_PRE 1
#define V_XT 0
#define V_MORE 0
#define V_CNT 4
constexpr int GM = 8;
constexpr unsigned WS_EPOCH = 0;
constexpr unsigned WS_FLAG = 4096;
enum { F_H = 0, F_O, F_X, F_Y, F_Z, F_N };
constexpr unsigned WS_G0 = WS_FLAG + F_N * 16 * 2048;
constexpr unsigned WS_G1 = WS_G0 + 256 * 512;
constexpr unsigned WS_MP = WS_G1 + 2048 * 16;
constexpr unsigned KVB = 1056 * 16;
constexpr unsigned WS_KVP = WS_MP + 64 * 8 * 512;
constexpr unsigned WS_KVS = WS_KVP + 256 * KVB;
constexpr unsigned WS_VP = WS_KVS + 16 * 16384;
constexpr unsigned WS_XS = WS_VP + 256 * 224 * 16;
constexpr unsigned WS_ZS = WS_XS + 256 * 2048;
constexpr unsigned WS_XF = WS_ZS + 256 * 2048;
constexpr unsigned WS_VF = WS_XF + 16 * 2048;
constexpr unsigned WS_HP = (WS_VF + 16 * 4096 + 65535) / 65536 * 65536;
constexpr unsigned WS_OP = WS_HP + S * D * 2;
constexpr unsigned WS_XP = WS_OP + S * D * 2;
constexpr unsigned WS_ZP = WS_XP + S * D * 2;
constexpr unsigned WS_YP = WS_ZP + S * FH * 2;
constexpr unsigned WS_END = WS_YP + S * FH * 2;
constexpr unsigned NSTW = 3;
constexpr unsigned WS_MARK = WS_END + 262144;
constexpr unsigned WS_STAMP = WS_END;
constexpr int SLOT = 16384;
constexpr int L_A = 0, L_B = 4 * SLOT, L_STG = 8 * SLOT, L_TAB = L_STG + 28672, L_TOTAL = 163840;
constexpr int Y_A = 28672, Y_B = 14336, Y_B0 = 0, Y_A0 = 3 * Y_B;
constexpr int T_RS = L_TAB, T_V1 = L_TAB + 1024, T_V2 = T_V1 + 896, T_GB = L_TAB + 2816  , T_CNT = L_TAB + 2944, T_X = L_TAB + 3072;
constexpr int G_RED = L_STG + 20480;
constexpr int G_CS = L_STG + 22528;
constexpr int G_SC = L_STG + 26112;
constexpr int G_KVF = L_STG + 17408;
constexpr int G_KT = L_STG + 16384;
static_assert(T_X + 1024 == L_TOTAL, "LDS map");

struct Params {
  const u16* c; const u16* wmod; char* ws; const u16* x; const u16* wqkv; const u16* wphi; const u16* wo; const u16* win;
  const u16* wsp; const u16* bsp; const u16* wout; u16* out;
};

DI i32x4 mk_srd(const void* p, unsigned bytes) {
  i32x4 r; const u64 a = (u64)p;
  r[0] = __builtin_amdgcn_readfirstlane((int)(unsigned)a);
  r[1] = __builtin_amdgcn_readfirstlane((int)((unsigned)(a >> 32) & 0xffffu));
  r[2] = __builtin_amdgcn_readfirstlane((int)bytes);
  r[3] = 0x00020000;
  asm volatile("s_nop 4" : "+s"(r));
  return r;
}
DI rsrc_t mk_rsrc(const void* p, unsigned bytes) { return __builtin_amdgcn_make_buffer_rsrc((void*)p, 0, bytes, 0x00020000); }
template <int OFF, bool SC1> DI void dma16(i32x4 srd, unsigned voff, unsigned soff) {
  if constexpr (SC1) asm volatile("buffer_load_dwordx4 %0, %1, %2 offen offset:%c3 sc1 lds" ::"v"(voff), "s"(srd), "s"(soff), "i"(OFF) : "memory");
  else asm volatile("buffer_load_dwordx4 %0, %1, %2 offen offset:%c3 lds" ::"v"(voff), "s"(srd), "s"(soff), "i"(OFF) : "memory");
}
template <int IMM> DI void set_m0(unsigned base) { asm volatile("s_add_u32 m0, %0, %c1\n\ts_nop 0" ::"s"(base), "i"(IMM) : "memory", "scc"); }
template <int N> DI void wait_vm() { asm volatile("s_waitcnt vmcnt(%0)" ::"n"(N) : "memory"); }
DI void wait_lgkm0() { asm volatile("" ::: "memory"); __builtin_amdgcn_s_waitcnt(0xC07F); asm volatile("" ::: "memory"); }
DI void bar() { SB(); asm volatile("s_barrier" ::: "memory"); SB(); }
DI void lbar() { wait_lgkm0(); bar(); }
template <int N> DI void bars() { static_for<0, N>([&](auto) { bar(); }); }
DI s16x4 trread(unsigned a) { return __builtin_amdgcn_ds_read_tr16_b64_v4i16((LAS s16x4*)a); }
template <class T> DI T lds_ld(unsigned a) { return *(LAS T*)a; }
template <class T> DI void lds_st(unsigned a, T v) { *(LAS T*)a = v; }
DI f32x4 mfma16(s16x8 a, s16x8 b, f32x4 c) { return __builtin_amdgcn_mfma_f32_16x16x32_bf16(__builtin_bit_cast(bf16x8, a), __builtin_bit_cast(bf16x8, b), c, 0, 0, 0); }
DI unsigned pk2(float a, float b) { f32x2 v = {a, b}; return __builtin_bit_cast(unsigned, __builtin_convertvector(v, bf16x2)); }
DI u32x2 pk4(f32x4 v) { u32x2 r = {pk2(v[0], v[1]), pk2(v[2], v[3])}; return r; }
DI s16x8 pk8(f32x4 a, f32x4 b) { u32x4 r = {pk2(a[0], a[1]), pk2(a[2], a[3]), pk2(b[0], b[1]), pk2(b[2], b[3])}; return __builtin_bit_cast(s16x8, r); }
DI float blo(unsigned w) { return __uint_as_float(w << 16); }
DI float bhi(unsigned w) { return __uint_as_float(w & 0xffff0000u); }
DI float bfr(float v) { return blo(pk2(v, v)); }
DI float vmax(float a, float b) { return __builtin_elementwise_maximum(a, b); }
DI float vmin(float a, float b) { return __builtin_elementwise_minimum(a, b); }
DI float x16_add(float v) { auto r = __builtin_amdgcn_permlane16_swap(__float_as_uint(v), __float_as_uint(v), false, false); return __uint_as_float(r[0]) + __uint_as_float(r[1]); }
DI float x32_add(float v) { auto r = __builtin_amdgcn_permlane32_swap(__float_as_uint(v), __float_as_uint(v), false, false); return __uint_as_float(r[0]) + __uint_as_float(r[1]); }
DI float x16_max(float v) { auto r = __builtin_amdgcn_permlane16_swap(__float_as_uint(v), __float_as_uint(v), false, false); return vmax(__uint_as_float(r[0]), __uint_as_float(r[1])); }
DI float x32_max(float v) { auto r = __builtin_amdgcn_permlane32_swap(__float_as_uint(v), __float_as_uint(v), false, false); return vmax(__uint_as_float(r[0]), __uint_as_float(r[1])); }
DI float x16_min(float v) { auto r = __builtin_amdgcn_permlane16_swap(__float_as_uint(v), __float_as_uint(v), false, false); return vmin(__uint_as_float(r[0]), __uint_as_float(r[1])); }
DI float x32_min(float v) { auto r = __builtin_amdgcn_permlane32_swap(__float_as_uint(v), __float_as_uint(v), false, false); return vmin(__uint_as_float(r[0]), __uint_as_float(r[1])); }
template <int CTRL> DI float dpp_add(float v) { return v + __uint_as_float(__builtin_amdgcn_update_dpp(0u, __float_as_uint(v), CTRL, 0xf, 0xf, true)); }
DI float row16_sum(float v) { v = dpp_add<0xB1>(v); v = dpp_add<0x4E>(v); v = dpp_add<0x141>(v); v = dpp_add<0x140>(v); return v; }
DI float wave_sum(float v) { return x32_add(x16_add(row16_sum(v))); }
DI u64 now() { return __builtin_amdgcn_s_memrealtime(); }
constexpr u64 DEADLINE = 3000000ull;

struct Ctx {
  rsrc_t ws; u32x2 tag; u64 t0; int lane, wid, tm, tn; unsigned lds0;
};
DI unsigned flag_off(int type, int group, int idx) { return (unsigned)(WS_FLAG + (type * 16 + group) * 2048 + idx * V_FSTR); }
template <int NW> DI void arrive(const Ctx& c, int type, int group, int idx) {
  if (c.lane == 0) {
    const unsigned o = __hip_atomic_fetch_add((LAS unsigned*)(c.lds0 + T_CNT + type * 4), 1u, __ATOMIC_RELAXED, __HIP_MEMORY_SCOPE_WORKGROUP);
    if (o == NW - 1) __builtin_amdgcn_raw_buffer_store_b64(c.tag, c.ws, flag_off(type, group, idx), 0, 16);
  }
}
DI void put4(const Ctx& c, unsigned off, f32x4 v) { u32x4 w = {pk2(v[0], v[1]), c.tag[0], pk2(v[2], v[3]), c.tag[1]}; __builtin_amdgcn_raw_buffer_store_b128(w, c.ws, off, 0, 16); }
DI void put2(const Ctx& c, unsigned off, float a, float b) { u32x4 v = {__float_as_uint(a), c.tag[0], __float_as_uint(b), c.tag[1]}; __builtin_amdgcn_raw_buffer_store_b128(v, c.ws, off, 0, 16); }
template <int N> struct Units {
  u32x4 v[N]; unsigned off, stride;
  DI void issue(const Ctx& c) {
#pragma unroll
    for (int i = 0; i < N; ++i) v[i] = __builtin_amdgcn_raw_buffer_load_b128(c.ws, off + i * stride, 0, 16);
  }
  template <int A, int B> DI void issue_part(const Ctx& c) {
#pragma unroll
    for (int i = A; i < B; ++i) v[i] = __builtin_amdgcn_raw_buffer_load_b128(c.ws, off + i * stride, 0, 16);
  }
  DI bool ok(const Ctx& c) {
    unsigned x = 0, y = 0;
#pragma unroll
    for (int i = 0; i < N; ++i) { asm volatile("" : "+v"(v[i])); x = __builtin_amdgcn_bitop3_b32(v[i][1], c.tag[0], x, 0xBE); y = __builtin_amdgcn_bitop3_b32(v[i][3], c.tag[1], y, 0xBE); }
    return __builtin_amdgcn_ballot_w64((x | y) != 0) == 0;
  }
  DI void finish(const Ctx& c) { if (__builtin_expect(!ok(c), 0)) { do { __builtin_amdgcn_s_sleep(6); issue(c); } while (!ok(c) && now() - c.t0 <= DEADLINE); } }
  DI float a(int i) const { return __uint_as_float(v[i][0]); }
  DI float b(int i) const { return __uint_as_float(v[i][2]); }
  DI f32x4 q(int i) const { f32x4 r = {blo(v[i][0]), bhi(v[i][0]), blo(v[i][2]), bhi(v[i][2])}; return r; }
};
template <int NI> DI void warm(i32x4 srd, unsigned voff, unsigned soff, unsigned step, unsigned dst) {
  soff = __builtin_amdgcn_readfirstlane(soff); asm volatile("s_nop 4" : "+s"(soff));
  set_m0<0>(dst);
#pragma unroll 4
  for (int i = 0; i < NI; ++i) dma16<0, false>(srd, voff, soff + i * step);
}
DI i32x4 code_srd() {
  u64 pc; unsigned off, size;
  asm volatile("s_getpc_b64 %0\n.Lcode_pc%=:\n\ts_mov_b32 %1, .Lcode_pc%=-fused_block\n\ts_mov_b32 %2, fused_block_end-fused_block" : "=s"(pc), "=s"(off), "=s"(size));
  return mk_srd((const void*)(pc - off), size < 0x20000u ? size : 0u);
}
DI unsigned voff8(int lane, unsigned rowb) { return (unsigned)(lane >> 3) * rowb + (lane & 7) * 16; }

#define STREF(i, role, label) tdummy
#define STAMP(i, role, label) do { } while (0)

template <int NT> DI int ktile(int i, int rot) {
  constexpr int HG = NT == 28 ? 14 : 8;
  const int g0 = rot >= 3 * HG ? 3 * HG : rot >= 2 * HG ? 2 * HG : rot >= HG ? HG : 0;
  if (V_ORD == 0) {
    if (NT == 32) return (i + rot) & 31;
    const int g = rot & ~(16 - 1), j = i - 1;
    return i == 0 ? rot : (g + j + (j >= rot - g ? 1 : 0)) & 15; }
  int k;
  if (i < HG) { k = rot + i; k = k >= g0 + HG ? k - HG : k; }
  else { k = g0 + i; k = k >= NT ? k - NT : k; }
  return k;
}
struct Src { i32x4 srd; unsigned voff[4]; unsigned base, pstep, kstep; int rot, skew = 0; };
template <int G, int NT, bool SC1> DI void issue(const Src& s, unsigned ring, int kt) {
  const int kk = ktile<NT>(NT == 16 ? (kt + s.skew) & 15 : kt, s.rot);
  unsigned so = __builtin_amdgcn_readfirstlane(s.base + (unsigned)kk * s.kstep); asm volatile("s_nop 4" : "+s"(so));
  const unsigned lb = ring + (unsigned)(kt & 3) * SLOT;
  static_for<0, (G + 3) / 4>([&](auto q) {
    constexpr int qi = q.value; const unsigned s2 = so + qi * s.pstep;
    set_m0<qi * 4096>(lb);
    dma16<0, SC1>(s.srd, s.voff[0], s2);
    dma16<1024, SC1>(s.srd, s.voff[1], s2);
    if constexpr (qi * 4 + 2 < G) { dma16<2048, SC1>(s.srd, s.voff[2], s2); dma16<3072, SC1>(s.srd, s.voff[3], s2); }
  });
}
template <int G, int NT, bool SC1> DI void issue4(const Src& s, unsigned ring) { issue<G, NT, SC1>(s, ring, 0); issue<G, NT, SC1>(s, ring, 1); issue<G, NT, SC1>(s, ring, 2); issue<G, NT, SC1>(s, ring, 3); }
struct Marks { unsigned v[6]; int n, lane;
  DI void mark(int w) {
  } };
template <int N> DI void wbar(Marks& mk) { wait_vm<N>(); mk.mark(0); bar(); mk.mark(1); }
template <int G, int NT, bool SC1, int GN, class Next> DI void load_rest(const Src& s, Next&& next, unsigned ring, Marks& mk, int k) {
  for (; k < NT - 4; ++k) { wbar<2 * G>(mk); issue<G, NT, SC1>(s, ring, k + 4); }
  wbar<2 * G>(mk); next(0);
  wbar<G + GN>(mk); next(1);
  wbar<2 * GN>(mk); next(2);
  wbar<63>(mk); next(3);
}
template <int G, int NT, bool SC1, int GN, int NTN, bool SC1N> DI void load_pass(const Src& s, const Src& n, unsigned ring, Marks& mk) {
  wbar<3 * G>(mk);
  load_rest<G, NT, SC1, GN>(s, [&](int i) { if constexpr (GN > 0) issue<GN, NTN, SC1N>(n, ring, i); }, ring, mk, 0);
}
template <int G, int NT, bool OWN, int GN, class Need, class Next>
DI void dep_pass(const Ctx& c, int type, int group, Need&& need, const Src& s, Next&& next, unsigned ring, Marks& mk) {
  const unsigned voff = flag_off(type, group, c.lane & 15);
  unsigned mask = 0; int nis = OWN ? 1 : 0;
  auto eval = [&](u32x2 v) { mask = (unsigned)__builtin_amdgcn_ballot_w64(v[0] == c.tag[0] && v[1] == c.tag[1]) & 0xffffu; };
  auto ready = [&](int i) { const unsigned nd = need(ktile<NT>(i, s.rot)); return (mask & nd) == nd; };
  auto force = [&](int upto) {
    while (nis <= upto) {
      while (!ready(nis)) {
        eval(__builtin_amdgcn_raw_buffer_load_b64(c.ws, voff, 0, 16));
        if (!ready(nis)) { if (now() - c.t0 > DEADLINE) mask = 0xffffu; else __builtin_amdgcn_s_sleep(V_CAD); }
      }
      asm volatile("" ::: "memory");
      issue<G, NT, true>(s, ring, nis); ++nis;
    }
  };
  auto more = [&](int upto) { while (nis <= upto && ready(nis)) { issue<G, NT, true>(s, ring, nis); ++nis; } };
  auto landed = [&](int t) { const int y = nis - 1 - t; if (y >= 3) wait_vm<3 * G + 1>(); else if (y == 2) wait_vm<2 * G + 1>(); else if (y == 1) wait_vm<G + 1>(); else wait_vm<1>(); };
  for (int i = 0; i < 100000 && *(volatile LAS unsigned*)(c.lds0 + T_CNT + type * 4) < (OWN ? (unsigned)V_CNT : 2u); ++i) __builtin_amdgcn_s_sleep(1);
  if (OWN && V_DLY) __builtin_amdgcn_s_sleep(V_DLY);
  int k = OWN ? 0 : -1;
  if (OWN) { force(1); if (V_MORE) more(3); mk.mark(0); bar(); mk.mark(1); }
  for (; k < NT - 4; ++k) {
    force(k + 1); more(k + 3 + (k < 0));
    if (mask == 0xffffu && k >= 0) break;
    u32x2 v = __builtin_amdgcn_raw_buffer_load_b64(c.ws, voff, 0, 16);
    landed(k + 1); mk.mark(0); bar(); mk.mark(1);
    asm volatile("" : "+v"(v));
    eval(v);
  }
  force(k + 3);
  load_rest<G, NT, true, GN>(s, next, ring, mk, k);
}
DI int mod3(int k) { return k - 3 * ((k * 11) >> 5); }
template <int G, bool SC1> DI void issue_at(const Src& s, unsigned so, unsigned lb) {
  so = __builtin_amdgcn_readfirstlane(so); asm volatile("s_nop 4" : "+s"(so));
  static_for<0, (G + 3) / 4>([&](auto q) {
    constexpr int qi = q.value; const unsigned s2 = so + qi * s.pstep;
    set_m0<qi * 4096>(lb);
    dma16<0, SC1>(s.srd, s.voff[0], s2); dma16<1024, SC1>(s.srd, s.voff[1], s2);
    if constexpr (qi * 4 + 2 < G) { dma16<2048, SC1>(s.srd, s.voff[2], s2); dma16<3072, SC1>(s.srd, s.voff[3], s2); }
  });
}
DI int swz_wide(int k) { return 2 * ((k & 3) | (((k >> 3) & 1) << 2)); }
DI int swz_narrow(int k) { return (((k >> 1) & 1) << 2) | (((k >> 3) & 1) << 1); }
DI Src src_packed(i32x4 srd, int lane, unsigned base, unsigned kstep, int rot) {
  Src s; s.srd = srd; s.base = base; s.pstep = 4096; s.kstep = kstep; s.rot = rot;
  for (int g = 0; g < 4; ++g) s.voff[g] = (unsigned)lane * 16;
  return s;
}
DI Src src_rows(i32x4 srd, int lane, unsigned base, unsigned rowb, int rot) {
  Src s; s.srd = srd; s.base = base; s.pstep = 32 * rowb; s.kstep = 128; s.rot = rot;
  for (int g = 0; g < 4; ++g) { const int row = g * 8 + (lane >> 3), c = (lane & 7) ^ ((row >> 1) & 7); s.voff[g] = (unsigned)row * rowb + c * 16 - g * 1024; }
  return s;
}
DI Src src_narrow(i32x4 srd, int lane, unsigned colb, unsigned rowb, int rot) {
  Src s; s.srd = srd; s.base = colb; s.pstep = 32 * rowb; s.kstep = 64 * rowb; s.rot = rot;
  for (int g = 0; g < 4; ++g) { const int k = g * 8 + (lane >> 3), c = (lane & 7) ^ swz_narrow(k); s.voff[g] = (unsigned)k * rowb + c * 16 - g * 1024; }
  return s;
}

enum { B_WIDE = 0, B_NARROW = 1, B_NK = 2 };
template <int FM, int FN, int BMODE, int NT, int NORM_FROM, bool FULL, class Tail>
DI void kloop(f32x4 (&acc)[FM][FN], const unsigned (&aaddr)[2], const unsigned (&bbase)[BMODE == B_NK ? 2 : FN], u64& tstart, Tail&& tail) {
  constexpr int NB = BMODE == B_NK ? 2 : FN;
  constexpr int RS = BMODE == B_WIDE ? 256 : 128;
  constexpr int RPH = FM + (BMODE == B_NK ? FN : 2 * FN), MPH = FM * FN;
  constexpr int NSET = FULL ? 4 : 2;
  s16x8 fa[NSET][FM], fb[NSET][FN]; s16x4 fbl[NSET][FN], fbh[NSET][FN];
  unsigned ca[2], cb[NB];
#pragma unroll
  for (int i = 0; i < FM; ++i)
#pragma unroll
    for (int j = 0; j < FN; ++j) { acc[i][j] = (f32x4)(0.f); asm volatile("" : "+v"(acc[i][j])); }
  auto rd = [&](auto SET, auto KSI, auto R) {
    constexpr int set = SET.value, ks = KSI.value, rr = R.value, NBR = RPH - FM;
    if constexpr (rr >= FM) {
      constexpr int x = rr - FM;
      if constexpr (BMODE == B_NK) fb[set][x] = lds_ld<s16x8>(cb[ks] + x * 2048);
      else { constexpr int j = x / 2, t = x % 2, o = (ks * 32 + t * 4) * RS;
        if constexpr (t == 0) fbl[set][j] = trread(cb[j] + o); else fbh[set][j] = trread(cb[j] + o); }
    } else fa[set][rr] = lds_ld<s16x8>(ca[ks] + rr * 2048);
  };
  auto mm = [&](auto SET, auto Q) {
    constexpr int set = SET.value, q = Q.value, i = q % FM, j = q / FM;
    s16x8 b; if constexpr (BMODE == B_NK) b = fb[set][j]; else b = __builtin_shufflevector(fbl[set][j], fbh[set][j], 0, 1, 2, 3, 4, 5, 6, 7);
    const s16x8 a = fa[set][i];
    if constexpr (j >= NORM_FROM) acc[i][j] = mfma16(a, b, acc[i][j]); else acc[i][j] = mfma16(b, a, acc[i][j]);
  };
  auto setb = [&](auto X, unsigned so) {
    constexpr int x = X.value;
    if constexpr (x < 2) { ca[x] = aaddr[x] + so; asm volatile("" : "+v"(ca[x])); }
    else if constexpr (x < 2 + NB) { cb[x - 2] = bbase[x - 2] + so; asm volatile("" : "+v"(cb[x - 2])); }
  };
  static_for<0, 2 + NB>([&](auto X) { setb(X, 0u); });
  lbar();
  if constexpr (FULL) {
    auto step = [&](auto C, auto N, unsigned so) {
      static_for<0, 2 * MPH>([&](auto Q) {
        constexpr int q = Q.value;
        mm(IC<C.value + q / MPH>{}, IC<q % MPH>{}); SB();
        static_for<0, 2>([&](auto U) { constexpr int r = q * 2 + U.value;
          if constexpr (r < 2 * RPH) rd(IC<N.value + r / RPH>{}, IC<r / RPH>{}, IC<r % RPH>{}); else setb(IC<r - 2 * RPH>{}, so); });
        SB();
      });
    };
    static_assert(4 * MPH >= 2 * RPH + 2 + NB && NT % 2 == 0, "slots");
    static_for<0, 2 * RPH>([&](auto R) { rd(IC<R.value / RPH>{}, IC<R.value / RPH>{}, IC<R.value % RPH>{}); });
    static_for<0, 2 + NB>([&](auto X) { setb(X, (unsigned)SLOT); });
    SB();
    static_for<0, 2 * MPH>([&](auto Q) { mm(IC<Q.value / MPH>{}, IC<Q.value % MPH>{}); });
    bar();
    static_for<0, 2 * RPH>([&](auto R) { rd(IC<2 + R.value / RPH>{}, IC<R.value / RPH>{}, IC<R.value % RPH>{}); });
    static_for<0, 2 + NB>([&](auto X) { setb(X, 2u * SLOT); });
    for (int kt = 1; kt < NT - 1; kt += 2) {
      wait_lgkm0(); bar(); SB();
      step(IC<2>{}, IC<0>{}, (unsigned)((kt + 2) & 3) * SLOT);
      wait_lgkm0(); bar(); SB();
      step(IC<0>{}, IC<2>{}, (unsigned)((kt + 3) & 3) * SLOT);
    }
    wait_lgkm0(); bar(); SB();
    tail(); SB();
    static_for<0, 2 * MPH>([&](auto Q) { mm(IC<2 + Q.value / MPH>{}, IC<Q.value % MPH>{}); });
  } else {
    constexpr int RPM = (RPH + 4 > MPH) ? 2 : 1;
    auto phase = [&](auto CS, auto NS, auto NKS, auto UPD, unsigned so) {
      constexpr int q0 = (RPH + RPM - 1) / RPM;
      static_for<0, MPH>([&](auto Q) {
        constexpr int q = Q.value;
        mm(CS, Q); SB();
        static_for<0, RPM>([&](auto U) { constexpr int r = q * RPM + U.value; if constexpr (r < RPH) rd(NS, NKS, IC<r>{}); });
        if constexpr (UPD.value && q >= q0) { setb(IC<(q - q0) * 2>{}, so); setb(IC<(q - q0) * 2 + 1>{}, so); }
        SB();
      });
      if constexpr (UPD.value) static_for<(MPH - q0) * 2, 2 + NB>([&](auto X) { setb(X, so); });
    };
    static_for<0, RPH>([&](auto R) { rd(IC<0>{}, IC<0>{}, R); });
    for (int kt = 0; kt < NT - 1; ++kt) {
      wait_lgkm0(); SB();
      phase(IC<0>{}, IC<1>{}, IC<1>{}, IC<1>{}, (unsigned)((kt + 1) & 3) * SLOT);
      wait_lgkm0(); bar(); SB();
      phase(IC<1>{}, IC<0>{}, IC<0>{}, IC<0>{}, 0u);
    }
    wait_lgkm0(); SB();
    phase(IC<0>{}, IC<1>{}, IC<1>{}, IC<0>{}, 0u);
    wait_lgkm0(); bar(); SB();
    tail(); SB();
    static_for<0, MPH>([&](auto Q) { mm(IC<1>{}, Q); });
  }
  SB();
}
DI void a_addrs(unsigned (&aaddr)[2], unsigned base, int row0, int lane) {
#pragma unroll
  for (int ks = 0; ks < 2; ++ks) { const int row = row0 + (lane & 15), kc = ks * 4 + (lane >> 4); aaddr[ks] = base + row * 128 + ((kc ^ ((row >> 1) & 7)) * 16); }
}
DI unsigned tr_addr(unsigned base, int chunk0  , bool wide, int lane) {
  const int q = (lane & 15) >> 2, p = lane & 3, k = 8 * (lane >> 4) + q, c = chunk0 + (p >> 1);
  return wide ? base + k * 256 + ((c ^ swz_wide(k)) * 16) + (p & 1) * 8 : base + k * 128 + ((c ^ swz_narrow(k)) * 16) + (p & 1) * 8;
}
constexpr float L2E = 1.4426950408889634f;
DI float max8(f32x4 a, f32x4 b) { return vmax(vmax(vmax(vmax(a[0], a[1]), a[2]), vmax(vmax(a[3], b[0]), b[1])), vmax(b[2], b[3])); }
DI float min8(f32x4 a, f32x4 b) { return vmin(vmin(vmin(vmin(a[0], a[1]), a[2]), vmin(vmin(a[3], b[0]), b[1])), vmin(b[2], b[3])); }
DI f32x4 exp2v(f32x4 v) { f32x4 r = {__builtin_amdgcn_exp2f(v[0]), __builtin_amdgcn_exp2f(v[1]), __builtin_amdgcn_exp2f(v[2]), __builtin_amdgcn_exp2f(v[3])}; return r; }
DI f32x4 rcpv(f32x4 v) { f32x4 r = {__builtin_amdgcn_rcpf(v[0]), __builtin_amdgcn_rcpf(v[1]), __builtin_amdgcn_rcpf(v[2]), __builtin_amdgcn_rcpf(v[3])}; return r; }
DI float hsum(f32x4 v) { return (v[0] + v[1]) + (v[2] + v[3]); }
template <class Op> DI float red4(float a, float b, float c, float d, Op op) {
  const auto p = __builtin_amdgcn_permlane16_swap(__float_as_uint(a), __float_as_uint(b), false, false), q = __builtin_amdgcn_permlane16_swap(__float_as_uint(c), __float_as_uint(d), false, false);
  const float ab = op(__uint_as_float(p[0]), __uint_as_float(p[1])), cd = op(__uint_as_float(q[0]), __uint_as_float(q[1]));
  const auto r = __builtin_amdgcn_permlane32_swap(__float_as_uint(ab), __float_as_uint(cd), false, false);
  return op(__uint_as_float(r[0]), __uint_as_float(r[1]));
}
DI void bcast4(float v, float (&o)[4]) {
  const auto h = __builtin_amdgcn_permlane32_swap(__float_as_uint(v), __float_as_uint(v), false, false);
  const auto p = __builtin_amdgcn_permlane16_swap(h[0], h[0], false, false), q = __builtin_amdgcn_permlane16_swap(h[1], h[1], false, false);
  o[0] = __uint_as_float(p[0]); o[1] = __uint_as_float(p[1]); o[2] = __uint_as_float(q[0]); o[3] = __uint_as_float(q[1]);
}
template <int NB> DI void softmax_pm(const f32x4 (&p)[NB][2], f32x4 (&e)[NB][4]) {
  static_assert(NB == 4, "four columns");
  float mx[4], mn[4], sp[4], sn[4];
#pragma unroll
  for (int n = 0; n < 4; ++n) { mx[n] = max8(p[n][0], p[n][1]); mn[n] = min8(p[n][0], p[n][1]); }
  SB();
  const float rx = red4(mx[0], mx[1], mx[2], mx[3], vmax) * -L2E, rn = red4(mn[0], mn[1], mn[2], mn[3], vmin) * L2E;
  bcast4(rx, mx); bcast4(rn, mn);
  SB();
#pragma unroll
  for (int n = 0; n < 4; ++n) { e[n][0] = p[n][0] * L2E + mx[n]; e[n][1] = p[n][1] * L2E + mx[n]; e[n][2] = p[n][0] * -L2E + mn[n]; e[n][3] = p[n][1] * -L2E + mn[n]; }
  SB();
#pragma unroll
  for (int n = 0; n < 4; ++n) { e[n][0] = exp2v(e[n][0]); e[n][1] = exp2v(e[n][1]); e[n][2] = exp2v(e[n][2]); e[n][3] = exp2v(e[n][3]); }
  SB();
#pragma unroll
  for (int n = 0; n < 4; ++n) { sp[n] = hsum(e[n][0] + e[n][1]); sn[n] = hsum(e[n][2] + e[n][3]); }
  SB();
  auto add = [](float a, float b) { return a + b; };
  const float ip = __builtin_amdgcn_rcpf(red4(sp[0], sp[1], sp[2], sp[3], add)), in = __builtin_amdgcn_rcpf(red4(sn[0], sn[1], sn[2], sn[3], add));
  bcast4(ip, sp); bcast4(in, sn);
  SB();
#pragma unroll
  for (int n = 0; n < 4; ++n) { e[n][0] *= sp[n]; e[n][1] *= sp[n]; e[n][2] *= sn[n]; e[n][3] *= sn[n]; }
}
template <int NJ> DI void gelu_t(f32x4 (&v)[NJ]) {
  const float k1 = -2.f * 0.7978845608028654f * L2E, k2 = k1 * 0.044715f;
  f32x4 t[NJ];
#pragma unroll
  for (int j = 0; j < NJ; ++j) t[j] = v[j] * v[j];
  SB();
#pragma unroll
  for (int j = 0; j < NJ; ++j) t[j] = t[j] * k2 + k1;
  SB();
#pragma unroll
  for (int j = 0; j < NJ; ++j) t[j] = t[j] * v[j];
  SB();
#pragma unroll
  for (int j = 0; j < NJ; ++j) t[j] = exp2v(t[j]);
  SB();
#pragma unroll
  for (int j = 0; j < NJ; ++j) t[j] = t[j] + 1.f;
  SB();
#pragma unroll
  for (int j = 0; j < NJ; ++j) t[j] = rcpv(t[j]);
  SB();
#pragma unroll
  for (int j = 0; j < NJ; ++j) v[j] = v[j] * t[j];
  SB();
}
DI f32x4 unpk4(u32x2 w) { f32x4 r = {blo(w[0]), bhi(w[0]), blo(w[1]), bhi(w[1])}; return r; }
DI s16x8 wphi_frag(const u16* wphi, int head, int ft, int lane) {
  const int f = 16 * ft + (lane & 15), g = lane >> 4; s16x8 r;
#pragma unroll
  for (int j = 0; j < 8; ++j) { const int d = (j < 4) ? 4 * g + j : 16 + 4 * g + j - 4; r[j] = (short)wphi[head * 1024 + d * 32 + f]; }
  return r;
}
template <int NJ> DI void row_stats(const f32x4 (&v)[2][NJ], f32x2 (&r)[2]) {
  f32x4 s[2]; float m[2], q[2];
#pragma unroll
  for (int i = 0; i < 2; ++i) { s[i] = v[i][0];
#pragma unroll
    for (int j = 1; j < NJ; ++j) s[i] += v[i][j]; }
#pragma unroll
  for (int i = 0; i < 2; ++i) m[i] = x16_add(hsum(s[i]));
#pragma unroll
  for (int i = 0; i < 2; ++i) m[i] = x32_add(m[i]) * (1.f / (16 * NJ));
#pragma unroll
  for (int i = 0; i < 2; ++i) { f32x4 d = v[i][0] - m[i]; s[i] = d * d;
#pragma unroll
    for (int j = 1; j < NJ; ++j) { d = v[i][j] - m[i]; s[i] += d * d; } }
#pragma unroll
  for (int i = 0; i < 2; ++i) q[i] = x16_add(hsum(s[i]));
#pragma unroll
  for (int i = 0; i < 2; ++i) { r[i][0] = m[i]; r[i][1] = x32_add(q[i]); }
}
DI float mean16(const Units<16>& p) {
  float t[8];
#pragma unroll
  for (int i = 0; i < 8; ++i) t[i] = p.a(i) + p.a(i + 8);
  return (((t[0] + t[1]) + (t[2] + t[3])) + ((t[4] + t[5]) + (t[6] + t[7]))) * (1.f / 16.f);
}
template <int NP> DI f32x2 combine16(const Units<16>& p, float mean) {
  float q[4] = {0.f, 0.f, 0.f, 0.f}, m2[4] = {0.f, 0.f, 0.f, 0.f};
#pragma unroll
  for (int t = 0; t < 16; ++t) { const float dm = p.a(t) - mean; q[t & 3] = fmaf(dm, dm, q[t & 3]); m2[t & 3] += p.b(t); }
  const float var = fmaf((q[0] + q[1]) + (q[2] + q[3]), (float)NP, (m2[0] + m2[1]) + (m2[2] + m2[3])) * (1.f / (16 * NP));
  const float rstd = __builtin_amdgcn_rsqf(var + 1e-5f);
  f32x2 r = {rstd, -mean * rstd}; return r;
}
DI float silu_bf(float v) { return bfr(v * __builtin_amdgcn_rcpf(1.f + __builtin_amdgcn_exp2f(-v * L2E))); }

extern "C" __global__ __launch_bounds__(512, 2) void fused_block(Params p) {
  extern __shared__ __attribute__((aligned(16))) char smem[];
  Ctx c;
  const int tid = threadIdx.x; c.lane = tid & 63; c.wid = __builtin_amdgcn_readfirstlane(tid >> 6);
  const int lane = c.lane, wid = c.wid;
  const int bid = blockIdx.x, xcd = bid & 7, idx = bid >> 3;
  constexpr int XM = 16 / GM, GN = 32 / GM;
  c.tm = (xcd % XM) * GM + idx % GM; c.tn = (xcd / XM) * GN + idx / GM;
  const int tm = c.tm, tn = c.tn;
  c.lds0 = (unsigned)(size_t)(LAS char*)smem; const unsigned lds0 = c.lds0;
  c.ws = mk_rsrc(p.ws, WS_END);
  c.t0 = now(); u64 tdummy;
  u32x2 ep;
  auto load_ep = [&]() { ep = __builtin_amdgcn_raw_buffer_load_b64(c.ws, 0u, WS_EPOCH, 16); };
  auto make_tag = [&]() {
    asm volatile("" : "+v"(ep));
    u64 e = ((u64)ep[1] << 32) | ep[0]; e += 0x9E3779B97F4A7C15ull; e = (e ^ (e >> 30)) * 0xBF58476D1CE4E5B9ull; e = (e ^ (e >> 27)) * 0x94D049BB133111EBull; e ^= e >> 31;
    c.tag[0] = __builtin_amdgcn_readfirstlane((unsigned)e); c.tag[1] = __builtin_amdgcn_readfirstlane((unsigned)(e >> 32));
  };
  auto warm_wsp = [&](i32x4 d, int q) { warm<8>(d, (unsigned)(lane >> 4) * 4096 + (lane & 15) * 16, (unsigned)(tm * 128 + q * 32) * 4096 + tn * 256, 4 * 4096, lds0 + T_X); };
  auto ln1_rows = [&](auto&& between) {
    u32x4 xr[2][2];
#pragma unroll
    for (int r = 0; r < 2; ++r) { const u16* q = p.x + (size_t)(tm * 128 + tn * 8 + (wid - 4) * 2 + r) * D + lane * 8; xr[r][0] = *(const u32x4*)q; xr[r][1] = *(const u32x4*)(q + 512); }
    load_ep();
    bar();
    between();
    make_tag();
    f32x4 xv[2][4]; float mean[2], q[2];
#pragma unroll
    for (int r = 0; r < 2; ++r) {
#pragma unroll
      for (int h = 0; h < 2; ++h) { u32x2 lo = {xr[r][h][0], xr[r][h][1]}, hi = {xr[r][h][2], xr[r][h][3]}; xv[r][2 * h] = unpk4(lo); xv[r][2 * h + 1] = unpk4(hi); }
      mean[r] = hsum((xv[r][0] + xv[r][1]) + (xv[r][2] + xv[r][3]));
    }
#pragma unroll
    for (int r = 0; r < 2; ++r) mean[r] = wave_sum(mean[r]) * (1.f / D);
#pragma unroll
    for (int r = 0; r < 2; ++r) { f32x4 s4 = (f32x4)(0.f);
#pragma unroll
      for (int h = 0; h < 4; ++h) { const f32x4 d = xv[r][h] - mean[r]; s4 += d * d; }
      q[r] = hsum(s4); }
#pragma unroll
    for (int r = 0; r < 2; ++r) q[r] = __builtin_amdgcn_rsqf(wave_sum(q[r]) * (1.f / D) + 1e-5f);
    if (lane < 2) { f32x2 v = {lane ? q[1] : q[0], lane ? -mean[1] * q[1] : -mean[0] * q[0]}; lds_st<f32x2>(lds0 + G_CS + 512 + ((wid - 4) * 2 + lane) * 8, v); }
    wait_lgkm0();
    unsigned arr = 0;
    if (lane == 0) arr = __hip_atomic_fetch_add((LAS unsigned*)(lds0 + T_CNT + 24), 1u, __ATOMIC_RELAXED, __HIP_MEMORY_SCOPE_WORKGROUP);
    if (__builtin_amdgcn_readfirstlane(arr) == 3 && lane < 8) {
      asm volatile("" ::: "memory");
      const f32x2 v = lds_ld<f32x2>(lds0 + G_CS + 512 + lane * 8);
      put2(c, WS_G1 + (unsigned)(tm * 128 + tn * 8 + lane) * 16, v[0], v[1]);
    }
  };
  auto warm0 = [&]() {
    const unsigned dst = lds0 + L_A + 3 * SLOT;
    warm<3>(code_srd(), (unsigned)lane * 16, (unsigned)idx * 3072, 1024, dst);
    const i32x4 d = mk_srd(p.wqkv, D * 3 * D * 2); const unsigned o = (unsigned)(64 * tm) * 6144 + 128 * tn;
    warm<8>(d, voff8(lane, 6144), o + 2048, 8 * 6144, dst); warm<8>(d, voff8(lane, 6144), o + 4096, 8 * 6144, dst); warm<8>(d, voff8(lane, 6144), o, 8 * 6144, dst);
  };
  if (wid >= 4) ln1_rows([&] {
    if (V_WQ == 1 && wid == 4) warm0();
    if (V_PRE && wid == 5) {
      const i32x4 wsd = mk_srd(p.ws, WS_END);
      warm<1>(wsd, lane < 8 ? flag_off(lane, lane == F_Z ? tn : tm, 0) : (unsigned)lane * (WS_END / 64 / 16 * 16), 0, 0, lds0 + T_RS);
    } });
  else {
  __builtin_amdgcn_s_setprio(3);
  {
    const int mr = (tm & 7) * 128 + wid * 32 + (lane >> 3);
    const u16* w0 = p.wmod + (size_t)mr * 6144 + (tm >> 3) * 1024 + 64 * tn + (lane & 7) * 8;
    u32x4 wa[4], xt[4]; unsigned cv[4];
#pragma unroll
    for (int i = 0; i < 4; ++i) wa[i] = __builtin_nontemporal_load((const u32x4*)(w0 + (size_t)i * 8 * 6144));
#pragma unroll
    for (int i = 0; i < 4; ++i) cv[i] = p.c[mr + 8 * i];
    load_ep();
    if (V_XT) {
#pragma unroll
    for (int h = 0; h < 4; ++h) xt[h] = *(const u32x4*)(p.x + (size_t)(tm * 128 + 32 * h + (tid >> 3)) * D + 64 * tn + (tid & 7) * 8);
    }
    if (tid < 8) lds_st<unsigned>(lds0 + T_CNT + tid * 4, 0u);
    lbar();
#pragma unroll
    for (int i = 0; i < 4; ++i) asm volatile("" : "+v"(cv[i]));
    float a[8];
#pragma unroll
    for (int e = 0; e < 8; ++e) a[e] = 0.f;
#pragma unroll
    for (int i = 0; i < 4; ++i) { const float sv = silu_bf(blo(cv[i]));
#pragma unroll
      for (int e = 0; e < 4; ++e) { a[2 * e] = fmaf(sv, blo(wa[i][e]), a[2 * e]); a[2 * e + 1] = fmaf(sv, bhi(wa[i][e]), a[2 * e + 1]); } }
#pragma unroll
    for (int e = 0; e < 8; ++e) a[e] = x32_add(x16_add(dpp_add<0x128>(a[e])));
    if (lane < 8) { f32x4 v0 = {a[0], a[1], a[2], a[3]}, v1 = {a[4], a[5], a[6], a[7]}; lds_st<f32x4>(lds0 + G_RED + wid * 256 + lane * 32, v0); lds_st<f32x4>(lds0 + G_RED + wid * 256 + lane * 32 + 16, v1); }
    make_tag();
    STAMP(1, "c", "P0 partial ready");
    wait_lgkm0();
    unsigned arr = 0;
    if (lane == 0) arr = __hip_atomic_fetch_add((LAS unsigned*)(lds0 + T_CNT + 28), 1u, __ATOMIC_RELAXED, __HIP_MEMORY_SCOPE_WORKGROUP);
    if (__builtin_amdgcn_readfirstlane(arr) == 3 && lane < 32) {
      asm volatile("" ::: "memory");
      f32x2 sum = lds_ld<f32x2>(lds0 + G_RED + lane * 8);
#pragma unroll
      for (int w = 1; w < 4; ++w) sum += lds_ld<f32x2>(lds0 + G_RED + w * 256 + lane * 8);
      put2(c, WS_G0 + (unsigned)((tn * 16 + tm) * 32 + lane) * 16, sum[0], sum[1]);
    }
    STAMP(2, "c", "P0 G0 out");
    if (!V_XT) {
#pragma unroll
    for (int h = 0; h < 4; ++h) xt[h] = *(const u32x4*)(p.x + (size_t)(tm * 128 + 32 * h + (tid >> 3)) * D + 64 * tn + (tid & 7) * 8);
    }
    Units<2> gm; gm.off = WS_G0 + (unsigned)((tn * 16 + (lane >> 2)) * 32 + wid * 8 + (lane & 3)) * 16; gm.stride = 64;
    Units<4> gs; gs.off = WS_G1 + (unsigned)(tm * 128 + (tid >> 3)) * 16; gs.stride = 32 * 16;
    __builtin_amdgcn_s_sleep(V_S1);
    gm.issue(c); gs.issue(c); gm.finish(c);
#pragma unroll
    for (int i = 0; i < 2; ++i) {
      const float m0 = bfr(x16_add(dpp_add<0x128>(dpp_add<0x124>(gm.a(i))))), m1 = bfr(x16_add(dpp_add<0x128>(dpp_add<0x124>(gm.b(i)))));
      if ((lane & 28) == 0) { f32x2 v = {lane < 32 ? m0 : bfr(1.f + m0), lane < 32 ? m1 : bfr(1.f + m1)}; lds_st<f32x2>(lds0 + G_CS + (lane >> 5) * 256 + (wid * 16 + i * 8 + (lane & 3) * 2) * 4, v); }
    }
    gs.finish(c);
    STAMP(3, "c", "P0 own sweep ok");
    lbar();
    STAMP(4, "c", "P0 b1");
    const f32x4 sa0 = lds_ld<f32x4>(lds0 + G_CS + (tid & 7) * 32), sa1 = lds_ld<f32x4>(lds0 + G_CS + (tid & 7) * 32 + 16);
    const f32x4 ca0 = lds_ld<f32x4>(lds0 + G_CS + 256 + (tid & 7) * 32), ca1 = lds_ld<f32x4>(lds0 + G_CS + 256 + (tid & 7) * 32 + 16);
#pragma unroll
    for (int h = 0; h < 4; ++h) {
      const u32x2 lo = {xt[h][0], xt[h][1]}, hi = {xt[h][2], xt[h][3]};
      const f32x4 t0 = unpk4(lo) * gs.a(h) + gs.b(h), t1 = unpk4(hi) * gs.a(h) + gs.b(h);
      const int row = h * 32 + (tid >> 3);
      const u32x4 hv = __builtin_bit_cast(u32x4, pk8(t0 * ca0 + sa0, t1 * ca1 + sa1));
      const unsigned o = row * 128 + (((tid & 7) ^ ((row >> 1) & 7)) * 16);
      __builtin_amdgcn_raw_buffer_store_b128(hv, c.ws, WS_HP + (unsigned)(tm * 16 + tn) * 16384 + o, 0, 16);
      lds_st<u32x4>(lds0 + L_A + o, hv);
    }
    STAMP(5, "c", "h stores issued");
    wait_vm<0>();
    STAMP(6, "c", "h drained");
    arrive<4>(c, F_H, tm, tn);
  }
  __builtin_amdgcn_s_setprio(0);
  }
  Marks mk; mk.n = 0; mk.lane = lane;
#pragma unroll
  for (int i = 0; i < 6; ++i) mk.v[i] = 0;
  if (wid == 4) {
    const unsigned ring = lds0 + L_A;
    __builtin_amdgcn_s_setprio(1);
    const i32x4 wsd = mk_srd(p.ws, WS_END);
    const Src sH = src_packed(wsd, lane, WS_HP + tm * 16 * 16384, 16384, tn);
    if (V_WQ == 0) warm0();
    bar();
    if (V_WQ == 2) warm0();
    auto one = [](int kk) { return 1u << kk; };
    Src sH2 = sH; sH2.skew = 12;
    dep_pass<16, 16, true, 0>(c, F_H, tm, one, sH, [](int) {}, ring, mk);
    bars<1>();
    load_pass<16, 16, true, 0, 16, true>(sH2, sH2, ring, mk);
    bars<1>();
    const Src sO = src_packed(wsd, lane, WS_OP + tm * 16 * 16384, 16384, tn);
    dep_pass<16, 16, true, 0>(c, F_O, tm, one, sO, [](int) {}, ring, mk);
    const Src sX = src_packed(wsd, lane, WS_XP + tm * 16 * 16384, 16384, tn);
    Src sX2 = sX; sX2.skew = 12;
    dep_pass<16, 16, true, 0>(c, F_X, tm, one, sX, [](int) {}, ring, mk);
    const Src sW = src_rows(mk_srd(p.wsp, S * S * 2), lane, (unsigned)tm * 128 * 4096, 4096, 16 * (tm >> 3));
    warm_wsp(sW.srd, 0);
    bars<1>();
    load_pass<16, 16, true, 16, 32, false>(sX2, sW, ring, mk);
    load_pass<16, 32, false, 0, 32, false>(sW, sW, ring, mk);
    {
      const Src sY = src_packed(wsd, lane, WS_YP + tm * 16 * Y_A, Y_A, tn);
      const unsigned voff = flag_off(F_Y, tm, lane & 15);
      unsigned mask = 0; int nis = 1;
      auto eval = [&](u32x2 v) { mask = (unsigned)__builtin_amdgcn_ballot_w64(v[0] == c.tag[0] && v[1] == c.tag[1]) & 0xffffu; };
      auto ready = [&](int i) { return (mask >> ktile<16>(i, tn)) & 1u; };
      auto go = [&]() { issue_at<28, true>(sY, sY.base + ktile<16>(nis, tn) * Y_A, lds0 + Y_A0 + mod3(nis - 1) * Y_A); ++nis; };
      auto force = [&](int upto) {
        while (nis <= upto) {
          while (!ready(nis)) { eval(__builtin_amdgcn_raw_buffer_load_b64(c.ws, voff, 0, 16)); if (!ready(nis)) { if (now() - c.t0 > DEADLINE) mask = 0xffffu; else __builtin_amdgcn_s_sleep(V_CAD); } }
          asm volatile("" ::: "memory");
          go();
        } };
      for (int i = 0; i < 100000 && *(volatile LAS unsigned*)(lds0 + T_CNT + F_Y * 4) < 4u; ++i) __builtin_amdgcn_s_sleep(1);
      force(1);
      mk.mark(0); bar(); mk.mark(1);
      int k = 0;
      for (; k < 13; ++k) {
        const int lim = k == 0 ? 3 : k + 2;
        force(k + 1);
        while (nis <= lim && ready(nis)) go();
        if (mask == 0xffffu && k > 0) break;
        u32x2 v = __builtin_amdgcn_raw_buffer_load_b64(c.ws, voff, 0, 16);
        const int y = nis - 2 - k; if (y >= 2) wait_vm<57>(); else if (y == 1) wait_vm<29>(); else wait_vm<1>();
        mk.mark(0); bar(); mk.mark(1);
        asm volatile("" : "+v"(v));
        eval(v);
      }
      force(k + 2);
      for (; k < 13; ++k) { wbar<28>(mk); go(); }
      wbar<28>(mk); wbar<0>(mk); wbar<63>(mk);
    }
    return;
  }
  if (wid == 5) {
    const unsigned ring = lds0 + L_B;
    __builtin_amdgcn_s_setprio(1);
    Src sB0;
    sB0.srd = mk_srd(p.wqkv, D * 3 * D * 2); sB0.base = 0; sB0.pstep = 16 * 6144; sB0.kstep = 64 * 6144; sB0.rot = tn;
    for (int g = 0; g < 4; ++g) { const int k = g * 4 + (lane >> 4), cc = (lane & 15) ^ swz_wide(k); const int col = 1024 + (cc >> 3) * 1024 + 64 * tn + 8 * (cc & 7); sB0.voff[g] = (unsigned)(k * 6144 + col * 2 - g * 1024); }
    auto own_h = [&]() { for (int i = 0; i < 4096 && *(volatile LAS unsigned*)(lds0 + T_CNT + F_H * 4) < 4; ++i) __builtin_amdgcn_s_sleep(2); };
    if (V_B0 == 0) issue4<16, 16, false>(sB0, ring);
    bar();
    if (V_B0 == 1) issue4<16, 16, false>(sB0, ring);
    own_h();
    if (V_B0 == 2) issue4<16, 16, false>(sB0, ring);
    const Src sWo = src_narrow(mk_srd(p.wo, D * D * 2), lane, 128 * tn, 2048, tn);
    warm<8>(sWo.srd, voff8(lane, 2048), (unsigned)(64 * tm) * 2048 + 128 * tn, 8 * 2048, lds0 + T_RS);
    Src sQ = src_narrow(sB0.srd, lane, 128 * tn, 6144, tn); sQ.skew = 12;
    load_pass<16, 16, false, 8, 16, false>(sB0, sQ, ring, mk);
    bars<1>();
    load_pass<8, 16, false, 8, 16, false>(sQ, sWo, ring, mk);
    bars<1>();
    Src sZ2; sZ2.srd = mk_srd(p.win, D * FF * 2); sZ2.base = FH * 2; sZ2.pstep = 16 * 7168; sZ2.kstep = 64 * 7168; sZ2.rot = tn;
    for (int g = 0; g < 4; ++g) { const int k = g * 4 + (lane >> 4), cc = ((lane & 15) ^ swz_wide(k)) - 2 * (tn & 1);
      sZ2.voff[g] = (cc >= 0 && cc < 14) ? (unsigned)(k * 7168 + (112 * tn + 8 * cc) * 2 - g * 1024) : 0x80000000u; }
    Src sZ1 = sZ2; sZ1.base = 0; sZ1.skew = 12;
    load_pass<8, 16, false, 16, 16, false>(sWo, sZ2, ring, mk);
    load_pass<16, 16, false, 16, 16, false>(sZ2, sZ1, ring, mk);
    warm_wsp(mk_srd(p.wsp, S * S * 2), 1);
    bars<1>();
    load_pass<16, 16, false, 0, 16, false>(sZ1, sZ1, ring, mk);
    const Src sZ = src_packed(mk_srd(p.ws, WS_END), lane, WS_ZP + tn * 32 * 14336, 14336, 16 * (tm >> 3));
    const Src sWout = src_narrow(mk_srd(p.wout, FH * D * 2), lane, 128 * tn, 2048, tn);
    {
      auto go = [&](int i) { issue_at<14, false>(sWout, sWout.base + ktile<16>(i, tn) * (112 * 2048), lds0 + Y_B0 + mod3(i) * Y_B); };
      dep_pass<14, 32, false, 14>(c, F_Z, tn, [](int kk) { return 1u << (kk >> 1); }, sZ, [&](int i) { if (i < 3) go(i); }, ring, mk);
      warm<14>(sWout.srd, voff8(lane, 2048), (unsigned)(112 * tm) * 2048 + 128 * tn, 8 * 2048, lds0 + T_V1);
      wbar<28>(mk);
      for (int k = 0; k < 13; ++k) { wbar<14>(mk); go(k + 3); }
      wbar<14>(mk); wbar<0>(mk); wbar<63>(mk);
    }
    return;
  }
  if (wid >= 6) {
    const int a = wid - 6, u = tm * 16 + tn;
    __builtin_amdgcn_s_setprio(3);
    const i32x4 csd = code_srd();
    auto code = [&](unsigned dst) { if (a == 0) warm<3>(csd, (unsigned)lane * 16, (unsigned)idx * 3072, 1024, dst); };
    bars<1 + 17>();
    u32x4 wi[16];
    {
      const u16* w2 = p.win + (size_t)(64 * tm + (lane >> 4)) * FF + (a ? 0 : FH) + 112 * tn + ((lane & 15) < 14 ? (lane & 15) : 0) * 8;
      code(lds0 + T_RS);
#pragma unroll
      for (int i = 0; i < 16; ++i) wi[i] = *(const u32x4*)(w2 + (size_t)i * 4 * FF);
    }
    bars<1>();
    {
      Units<8> pv; pv.off = WS_KVP + (unsigned)(tn * 16 + (lane >> 5) * 8) * KVB + (unsigned)(tm * 64 + a * 32 + (lane & 31)) * 16; pv.stride = KVB;
      Units<4> zv; zv.off = WS_KVP + (unsigned)(tn * 16 + (lane >> 4) * 4) * KVB + (unsigned)(1024 + a * 16 + (lane & 15)) * 16; zv.stride = KVB;
      bars<4>();
      pv.template issue_part<0, 4>(c); bar(); pv.template issue_part<4, 8>(c); bar();
      zv.issue(c);
      bars<4>();
      STAMP(7, "h", "KV wait begin"); pv.finish(c); STAMP(8, "h", "KV seen");
      f32x4 sum = ((pv.q(0) + pv.q(1)) + (pv.q(2) + pv.q(3))) + ((pv.q(4) + pv.q(5)) + (pv.q(6) + pv.q(7)));
      asm volatile("" : "+v"(sum));
      bars<1>();
#pragma unroll
      for (int e = 0; e < 4; ++e) sum[e] = x32_add(sum[e]);
      if (lane < 32) put4(c, WS_KVS + (unsigned)tn * 16384 + (unsigned)(tm * 64 + a * 32 + lane) * 16, sum);
      bars<1>();
      zv.finish(c);
      sum = (zv.q(0) + zv.q(1)) + (zv.q(2) + zv.q(3));
      asm volatile("" : "+v"(sum));
      bars<1>();
#pragma unroll
      for (int e = 0; e < 4; ++e) sum[e] = x32_add(x16_add(sum[e]));
      if (lane < 16) { const int ft = lane >> 2, g = lane & 3; lds_st<u32x2>(lds0 + G_KVF + a * 4224 + 4096 + ((ft >> 1) * 4 + g) * 16 + (ft & 1) * 8, pk4(sum)); }
      Units<8> kv; kv.off = WS_KVS + (unsigned)tn * 16384 + (unsigned)(a * 512 + lane) * 16; kv.stride = 1024;
      bars<4>();
      kv.issue(c);
      STAMP(9, "h", "KS wait begin"); kv.finish(c); STAMP(10, "h", "KS seen");
#pragma unroll
      for (int i = 0; i < 8; ++i) { const int ft = i >> 1, dt = i & 1; u32x2 v = {kv.v[i][0], kv.v[i][2]}; lds_st<u32x2>(lds0 + G_KVF + a * 4224 + ((dt * 2 + (ft >> 1)) * 64 + lane) * 16 + (ft & 1) * 8, v); }
    }
    {
      const int cbk = u >> 2, r0 = (u & 3) * 256 + a * 128;
      const u16* w = p.wmod + (size_t)(r0 + (lane >> 3)) * 6144 + 2048 + cbk * 64 + (lane & 7) * 8;
      const unsigned cw = *(const unsigned*)(p.c + r0 + 2 * lane);
      u32x4 wv[16]; float sv[16];
#pragma unroll
      for (int i = 0; i < 16; ++i) wv[i] = __builtin_nontemporal_load((const u32x4*)(w + (size_t)i * 8 * 6144));
      code(lds0 + T_RS);
      lbar();
      { f32x2 v = {silu_bf(blo(cw)), silu_bf(bhi(cw))}; lds_st<f32x2>(lds0 + G_SC + a * 512 + lane * 8, v); }
#pragma unroll
      for (int i = 0; i < 16; ++i) sv[i] = lds_ld<float>(lds0 + G_SC + a * 512 + ((lane >> 3) + 8 * i) * 4);
      float acc[8];
#pragma unroll
      for (int e = 0; e < 8; ++e) acc[e] = 0.f;
#pragma unroll
      for (int i = 0; i < 16; ++i)
#pragma unroll
        for (int e = 0; e < 4; ++e) { acc[2 * e] = fmaf(sv[i], blo(wv[i][e]), acc[2 * e]); acc[2 * e + 1] = fmaf(sv[i], bhi(wv[i][e]), acc[2 * e + 1]); }
#pragma unroll
      for (int e = 0; e < 8; ++e) acc[e] = x32_add(x16_add(dpp_add<0x128>(acc[e])));
      const float s0 = (lane & 16) ? ((lane & 8) ? acc[6] : acc[4]) : ((lane & 8) ? acc[2] : acc[0]), s1 = (lane & 16) ? ((lane & 8) ? acc[7] : acc[5]) : ((lane & 8) ? acc[3] : acc[1]);
      if (lane < 32) put2(c, WS_MP + (unsigned)((cbk * 8 + (u & 3) * 2 + a) * 32 + (lane & 7) * 4 + (lane >> 3)) * 16, s0, s1);
    }
    {

      Units<8> m0, m1;
      const int b0 = a == 0 ? (lane < 32 ? tn : 32 + tn) : (lane < 32 ? 32 + tm : 16 + tm);
      m0.off = WS_MP + (unsigned)(b0 * 256 + (lane & 31)) * 16; m0.stride = 512;
      m1.off = WS_MP + (unsigned)((48 + tn) * 256 + (lane & 31)) * 16; m1.stride = 512;
      bars<2>();
      m0.template issue_part<0, 4>(c); bar(); m0.template issue_part<4, 8>(c); bar();
      if (a == 0) m1.template issue_part<0, 4>(c);
      bar();
      if (a == 0) m1.template issue_part<4, 8>(c);
      bars<5>();
      m0.finish(c);
      float s0 = 0.f, s1 = 0.f;
#pragma unroll
      for (int i = 0; i < 8; ++i) { s0 += m0.a(i); s1 += m0.b(i); }
      s0 = bfr(s0); s1 = bfr(s1);
      if (a == 0) {
        if (lane >= 32) { s0 = bfr(1.f + s0); s1 = bfr(1.f + s1); }
        f32x2 v = {s0, s1}; lds_st<f32x2>(lds0 + T_X + lane * 8, v);
        bar();
        m1.finish(c);
        s0 = 0.f; s1 = 0.f;
#pragma unroll
        for (int i = 0; i < 8; ++i) { s0 += m1.a(i); s1 += m1.b(i); }
        if (lane < 32) lds_st<unsigned>(lds0 + T_GB + lane * 4, pk2(s0, s1));
      } else {
        if (lane < 32) { s0 = bfr(1.f + s0); s1 = bfr(1.f + s1); }
        lds_st<float>(lds0 + G_KT + (lane & 31) * 16 + (lane >> 5) * 4, s0); lds_st<float>(lds0 + G_KT + (lane & 31) * 16 + 8 + (lane >> 5) * 4, s1);
        bar();
      }
      STAMP(11, "h", "mod vec ready");
      lbar();
      bars<5>();
    }
    {
      const int rg = lane >> 4;
      f32x2 sk[16];
#pragma unroll
      for (int i = 0; i < 16; ++i) sk[i] = lds_ld<f32x2>(lds0 + G_KT + (rg + 4 * i) * 8);
      f32x4 v1[2], v2[2];
#pragma unroll
      for (int e = 0; e < 2; ++e) { v1[e] = (f32x4)(0.f); v2[e] = (f32x4)(0.f); }
#pragma unroll
      for (int i = 0; i < 16; ++i) {
        const u32x2 lo = {wi[i][0], wi[i][1]}, hi = {wi[i][2], wi[i][3]}; const f32x4 l = unpk4(lo), h = unpk4(hi);
        v1[0] += l * sk[i][0]; v1[1] += h * sk[i][0]; v2[0] += l * sk[i][1]; v2[1] += h * sk[i][1];
      }
#pragma unroll
      for (int e = 0; e < 2; ++e)
#pragma unroll
        for (int r = 0; r < 4; ++r) { v1[e][r] = x32_add(x16_add(v1[e][r])); v2[e][r] = x32_add(x16_add(v2[e][r])); }
      const f32x4 p1 = (rg & 2) ? v1[1] : v1[0], p2 = (rg & 2) ? v2[1] : v2[0];
      const unsigned off = WS_VP + (unsigned)((tn * 16 + tm) * 224 + a * 56 + (lane & 15) * 4 + rg) * 16;
      if ((lane & 15) < 14) { put2(c, off, (rg & 1) ? p1[2] : p1[0], (rg & 1) ? p1[3] : p1[1]); put2(c, off + 112 * 16, (rg & 1) ? p2[2] : p2[0], (rg & 1) ? p2[3] : p2[1]); }
    }
    {
      Units<2> t; t.off = WS_XS + (unsigned)((tm * 16 + (lane >> 2)) * 128 + tn * 8 + (lane & 3)) * 16; t.stride = 64;
      Units<4> l1; l1.off = WS_VP + (unsigned)((tn * 16 + (lane >> 4)) * 224 + tm * 14 + ((lane & 15) < 14 ? (lane & 15) : 0)) * 16; l1.stride = 4 * 224 * 16;
      if (a == 0) {
        bars<3>(); t.issue(c); bars<4>(); t.finish(c);
        auto sum16 = [](float v) { return x32_add(x16_add(dpp_add<0x128>(dpp_add<0x124>(v)))); };
        float r0[2], r1[2];
#pragma unroll
        for (int i = 0; i < 2; ++i) {
          const float mean = sum16(t.a(i)) * (1.f / 16.f), dm = t.a(i) - mean;
          r0[i] = __builtin_amdgcn_rsqf(sum16(fmaf(dm * dm, 64.f, t.b(i))) * (1.f / 1024.f) + 1e-5f); r1[i] = -mean * r0[i];
        }
        if (lane < 8) put2(c, WS_XF + (unsigned)(tm * 128 + tn * 8 + lane) * 16, lane < 4 ? r0[0] : r0[1], lane < 4 ? r1[0] : r1[1]);
      } else {
        bars<3>(); l1.issue(c); bars<4>(); l1.finish(c);
        const float s0 = x32_add(x16_add((l1.a(0) + l1.a(1)) + (l1.a(2) + l1.a(3)))), s1 = x32_add(x16_add((l1.b(0) + l1.b(1)) + (l1.b(2) + l1.b(3))));
        if (lane < 16) put2(c, WS_VF + (unsigned)((tn * 16 + tm) * 16 + lane) * 16, s0, s1);
      }
      bars<4>();
      Units<1> gx; gx.off = WS_XF + (unsigned)(tm * 128 + a * 64 + lane) * 16; gx.stride = 0;
      Units<2> gv; gv.off = WS_VF + (unsigned)(tn * 256 + a * 128 + lane) * 16; gv.stride = 1024;
      gx.issue(c); gv.issue(c);
      bars<4>();
      gx.finish(c); gv.finish(c);
      { f32x2 v = {gx.a(0), gx.b(0)}; lds_st<f32x2>(lds0 + T_RS + (a * 64 + lane) * 8, v); }
#pragma unroll
      for (int i = 0; i < 2; ++i) { f32x2 v = {gv.a(i), gv.b(i)}; if ((lane & 15) < 14) lds_st<f32x2>(lds0 + T_V1 + ((a * 8 + i * 4 + (lane >> 4)) * 14 + (lane & 15)) * 8, v); }
      lbar();
      bar();
    }
    code(lds0 + T_X);
    warm_wsp(mk_srd(p.wsp, S * S * 2), 2 + a);
    bars<1>();
    {
      Units<16> ps; ps.off = WS_ZS + (unsigned)(tm * 16 * 128 + a * 64 + lane) * 16; ps.stride = 2048;
      const int g = lane >> 4, i16 = lane & 15;
      u32x2 ul[2][7], uh[2][7];
      bars<2>();
      ps.issue(c);
#pragma unroll
      for (int cq = 0; cq < 2; ++cq) {
        bar();
        const unsigned ra = lds0 + L_STG + (a * 64 + (cq * 4 + g) * 8 + (i16 >> 2)) * 224 + (i16 & 3) * 8;
#pragma unroll
        for (int ft = 0; ft < 7; ++ft) { ul[cq][ft] = __builtin_bit_cast(u32x2, trread(ra + ft * 32)); uh[cq][ft] = __builtin_bit_cast(u32x2, trread(ra + ft * 32 + 4 * 224)); }
      }
      bar();
      STAMP(13, "h", "ZS wait begin"); ps.finish(c); STAMP(14, "h", "ZS seen");
      float mean = mean16(ps); asm volatile("" : "+v"(mean));
      bar();
      { const f32x2 r = combine16<112>(ps, mean); lds_st<float>(lds0 + T_X + (a * 64 + lane) * 4, r[0]); lds_st<float>(lds0 + T_X + 512 + (a * 64 + lane) * 4, r[1]); }
      wait_lgkm0();
      f32x4 rs[2][2], nb[2][2];
#pragma unroll
      for (int cq = 0; cq < 2; ++cq)
#pragma unroll
        for (int e = 0; e < 2; ++e) { const unsigned o = lds0 + T_X + (a * 64 + (cq * 4 + g) * 8 + 4 * e) * 4; rs[cq][e] = lds_ld<f32x4>(o); nb[cq][e] = lds_ld<f32x4>(o + 512); }
      bar();
      const unsigned zo = WS_ZP + (unsigned)(tn * 32 + tm * 2 + a) * 14336 + i16 * 128;
#pragma unroll
      for (int n = 0; n < 14; ++n) {
        const int cq = n / 7, ft = n % 7;
        __builtin_amdgcn_raw_buffer_store_b128(__builtin_bit_cast(u32x4, pk8(unpk4(ul[cq][ft]) * rs[cq][0] + nb[cq][0], unpk4(uh[cq][ft]) * rs[cq][1] + nb[cq][1])), c.ws,
                                               zo + ft * 2048 + (((cq * 4 + g) ^ (i16 >> 1)) * 16), 0, 16);
        if (n & 1) bar();
      }
      bars<2>();
      wait_vm<0>(); arrive<2>(c, F_Z, tn, tm); STAMP(15, "h", "Z flag");
      bars<1>();
    }
    {
      const float b = blo(p.bsp[tm * 128 + a * 64 + lane]);
      bars<2>();
      lds_st<float>(lds0 + T_X + (a * 64 + lane) * 4, b);
      lbar();
      bars<30>();
    }
    bars<17>();
    if (u == 0 && a == 0 && lane == 0) {
      const u64 e = (((u64)ep[1] << 32) | ep[0]) + 1; u32x2 v = {(unsigned)e, (unsigned)(e >> 32)};
      __builtin_amdgcn_raw_buffer_store_b64(v, c.ws, 0u, WS_EPOCH, 16);
    }
    return;
  }

  const int g = lane >> 4, i16 = lane & 15;
  unsigned aaddr[2];
  s16x8 wph[2][2];
#pragma unroll
  for (int hh = 0; hh < 2; ++hh) { wph[hh][0] = wphi_frag(p.wphi, 2 * tn + hh, 0, lane); wph[hh][1] = wphi_frag(p.wphi, 2 * tn + hh, 1, lane); }
  {
    const int wm = wid >> 1, wn = wid & 1;
    unsigned bb[4];
    a_addrs(aaddr, lds0 + L_A, 64 * wm, lane);
#pragma unroll
    for (int j = 0; j < 4; ++j) bb[j] = tr_addr(lds0 + L_B, (j >> 1) * 8 + 4 * wn + 2 * (j & 1), true, lane);
    f32x4 acc[4][4];
    kloop<4, 4, B_WIDE, 16, 2, false>(acc, aaddr, bb, STREF(7, "c", "P1a start"), [] {});
    STAMP(8, "c", "P1a loop end");
    const s16x8 wk0 = wn ? wph[1][0] : wph[0][0], wk1 = wn ? wph[1][1] : wph[0][1];
    f32x4 pk[4][2], e[4][4];
#pragma unroll
    for (int i = 0; i < 4; ++i) { const s16x8 kf = pk8(acc[i][0], acc[i][1]); pk[i][0] = mfma16(wk0, kf, (f32x4)(0.f)); pk[i][1] = mfma16(wk1, kf, (f32x4)(0.f)); }
    s16x8 vf[2][2];
#pragma unroll
    for (int hb = 0; hb < 2; ++hb) { vf[hb][0] = pk8(acc[2 * hb][2], acc[2 * hb + 1][2]); vf[hb][1] = pk8(acc[2 * hb][3], acc[2 * hb + 1][3]); }
    SB();
    softmax_pm<4>(pk, e);
    const unsigned tb = lds0 + L_B + wid * SLOT + 8192, tw = tb + i16 * 128 + g * 8, tsw = (i16 >> 1) & 3;
#pragma unroll
    for (int i = 0; i < 4; ++i)
#pragma unroll
      for (int ft = 0; ft < 4; ++ft) lds_st<u32x2>(tw + i * 2048 + ((ft ^ tsw) * 32), pk4(e[i][ft]));
    wait_lgkm0();
    const int rr = 4 * g + (i16 >> 2);
    const unsigned tr = tb + rr * 128 + (i16 & 3) * 8, trs = (rr >> 1) & 3;
    s16x4 lo[2][4], hi[2][4];
#pragma unroll
    for (int hb = 0; hb < 2; ++hb)
#pragma unroll
      for (int ft = 0; ft < 4; ++ft) { lo[hb][ft] = trread(tr + hb * 4096 + ((ft ^ trs) * 32)); hi[hb][ft] = trread(tr + hb * 4096 + 2048 + ((ft ^ trs) * 32)); }
    f32x4 kv[4][3];
    const s16x8 ones = {0x3f80, 0x3f80, 0x3f80, 0x3f80, 0x3f80, 0x3f80, 0x3f80, 0x3f80};
#pragma unroll
    for (int hb = 0; hb < 2; ++hb)
#pragma unroll
      for (int ft = 0; ft < 4; ++ft) {
        const s16x8 pf = __builtin_shufflevector(lo[hb][ft], hi[hb][ft], 0, 1, 2, 3, 4, 5, 6, 7);
        kv[ft][0] = mfma16(pf, vf[hb][0], hb ? kv[ft][0] : (f32x4)(0.f)); kv[ft][1] = mfma16(pf, vf[hb][1], hb ? kv[ft][1] : (f32x4)(0.f)); kv[ft][2] = mfma16(pf, ones, hb ? kv[ft][2] : (f32x4)(0.f));
      }
    STAMP(9, "c", "P1a softmax+kv done");
    auto fin = [&](auto WM) {
      constexpr int K0 = 2 * WM.value, S0 = 2 - K0;
      const unsigned po = lds0 + L_STG + wid * 6144 + lane * 16, pi = lds0 + L_STG + (wid ^ 2) * 6144 + lane * 16;
#pragma unroll
      for (int f = 0; f < 2; ++f)
#pragma unroll
        for (int t = 0; t < 3; ++t) lds_st<f32x4>(po + (f * 3 + t) * 1024, kv[S0 + f][t]);
      lbar();
      f32x4 o[2][3];
#pragma unroll
      for (int f = 0; f < 2; ++f)
#pragma unroll
        for (int t = 0; t < 3; ++t) o[f][t] = lds_ld<f32x4>(pi + (f * 3 + t) * 1024);
      const unsigned dst = WS_KVP + (unsigned)(tn * 16 + tm) * KVB;
#pragma unroll
      for (int f = 0; f < 2; ++f) { const unsigned o0 = dst + (unsigned)(wn * 512 + (K0 + f) * 128 + lane) * 16; put4(c, o0, o[f][0] + kv[K0 + f][0]); put4(c, o0 + 1024, o[f][1] + kv[K0 + f][1]); }
      if (i16 < 2) put4(c, dst + (unsigned)(1024 + wn * 16 + (K0 + i16) * 4 + g) * 16, i16 ? o[1][2] + kv[K0 + 1][2] : o[0][2] + kv[K0][2]);
    };
    if (wm == 0) fin(IC<0>{}); else fin(IC<1>{});
  }
  STAMP(10, "c", "P1a epi end");
  a_addrs(aaddr, lds0 + L_A, 32 * wid, lane);
  unsigned bn[4];
#pragma unroll
  for (int j = 0; j < 4; ++j) bn[j] = tr_addr(lds0 + L_B, 2 * j, false, lane);
  const unsigned sg = lds0 + L_A + wid * 4096;
  auto put_rows = [&](unsigned dst) {
    wait_lgkm0();
    u32x4 v[4];
#pragma unroll
    for (int i = 0; i < 4; ++i) v[i] = lds_ld<u32x4>(sg + i * 1024 + lane * 16);
#pragma unroll
    for (int i = 0; i < 4; ++i) __builtin_amdgcn_raw_buffer_store_b128(v[i], c.ws, dst + wid * 4096 + i * 1024 + lane * 16, 0, 16);
  };
  auto img = [&](int i, int j) { return sg + (16 * i + i16) * 128 + (((2 * j + (g >> 1)) ^ (i16 >> 1)) * 16) + (g & 1) * 8; };
  {
    f32x4 acc[2][4];
    kloop<2, 4, B_NARROW, 16, 99, true>(acc, aaddr, bn, STREF(11, "c", "P1b start"), [] {});
    STAMP(12, "c", "P1b loop end");
    f32x4 pq[4][2], e[4][4];
#pragma unroll
    for (int n = 0; n < 4; ++n) { const int i = n >> 1, hh = n & 1; const s16x8 qf = pk8(acc[i][2 * hh], acc[i][2 * hh + 1]); pq[n][0] = mfma16(wph[hh][0], qf, (f32x4)(0.f)); pq[n][1] = mfma16(wph[hh][1], qf, (f32x4)(0.f)); }
    SB();
    softmax_pm<4>(pq, e);
    s16x8 fp[4], fn[4];
#pragma unroll
    for (int n = 0; n < 4; ++n) { fp[n] = pk8(e[n][0], e[n][1]); fn[n] = pk8(e[n][2], e[n][3]); asm volatile("" : "+v"(fp[n]), "+v"(fn[n])); }
    SB();
    STAMP(13, "c", "phi_q done");
    bar();
    STAMP(14, "c", "kv ready");
    s16x8 kf[2][6];
#pragma unroll
    for (int hh = 0; hh < 2; ++hh) { const unsigned kb = lds0 + G_KVF + hh * 4224;
#pragma unroll
      for (int t = 0; t < 4; ++t) kf[hh][t] = lds_ld<s16x8>(kb + t * 1024 + lane * 16);
      kf[hh][4] = lds_ld<s16x8>(kb + 4096 + g * 16); kf[hh][5] = lds_ld<s16x8>(kb + 4096 + 64 + g * 16); }
    f32x4 n0[4], n1[4], dn[4];
#pragma unroll
    for (int n = 0; n < 4; ++n) { const int hh = n & 1; dn[n] = mfma16(kf[hh][4], fp[n], (f32x4)(0.f)); n0[n] = mfma16(kf[hh][0], fp[n], (f32x4)(0.f)); n1[n] = mfma16(kf[hh][2], fp[n], (f32x4)(0.f)); }
#pragma unroll
    for (int n = 0; n < 4; ++n) { const int hh = n & 1; dn[n] = mfma16(kf[hh][5], fn[n], dn[n]); n0[n] = mfma16(kf[hh][1], fn[n], n0[n]); n1[n] = mfma16(kf[hh][3], fn[n], n1[n]); }
#pragma unroll
    for (int n = 0; n < 4; ++n) { const int i = n >> 1, hh = n & 1; const float r = __builtin_amdgcn_rcpf(dn[n][0] + 1e-6f);
      lds_st<u32x2>(img(i, 2 * hh), pk4(n0[n] * r)); lds_st<u32x2>(img(i, 2 * hh + 1), pk4(n1[n] * r)); }
    put_rows(WS_OP + (unsigned)(tm * 16 + tn) * 16384);
    STAMP(15, "c", "o stores issued");
    wait_vm<0>(); arrive<4>(c, F_O, tm, tn);
  }
  STAMP(16, "c", "o flag");
  u32x2 x1p[2][4];
  {
    u32x2 xin[2][4];
#pragma unroll
    for (int i = 0; i < 2; ++i)
#pragma unroll
      for (int j = 0; j < 4; ++j) xin[i][j] = *(const u32x2*)(p.x + (size_t)(tm * 128 + 32 * wid + 16 * i + i16) * D + 64 * tn + 16 * j + 4 * g);
    f32x4 acc[2][4], ga[4], cb[4];
    kloop<2, 4, B_NARROW, 16, 99, true>(acc, aaddr, bn, STREF(17, "c", "P3 start"), [&] {
#pragma unroll
      for (int j = 0; j < 4; ++j) { ga[j] = lds_ld<f32x4>(lds0 + T_X + (16 * j + 4 * g) * 4); cb[j] = lds_ld<f32x4>(lds0 + T_X + 256 + (16 * j + 4 * g) * 4); }
    });
    STAMP(18, "c", "P3 loop end");
    f32x4 x1[2][4];
#pragma unroll
    for (int i = 0; i < 2; ++i)
#pragma unroll
      for (int j = 0; j < 4; ++j) x1p[i][j] = pk4(unpk4(xin[i][j]) + ga[j] * acc[i][j]);
    SB();
#pragma unroll
    for (int i = 0; i < 2; ++i)
#pragma unroll
      for (int j = 0; j < 4; ++j) { x1[i][j] = unpk4(x1p[i][j]); lds_st<u32x2>(img(i, j), pk4(x1[i][j] * cb[j])); }
    put_rows(WS_XP + (unsigned)(tm * 16 + tn) * 16384);
    STAMP(19, "c", "x stores issued");
    f32x2 stt[2]; row_stats<4>(x1, stt);
#pragma unroll
    for (int i = 0; i < 2; ++i)
#pragma unroll
      for (int j = 0; j < 4; ++j) asm volatile("" : "+v"(x1p[i][j]));
    wait_vm<0>(); arrive<4>(c, F_X, tm, tn);
    if (g < 2) put2(c, WS_XS + (unsigned)((tm * 16 + tn) * 128 + 32 * wid + 16 * g + i16) * 16, g ? stt[1][0] : stt[0][0], g ? stt[1][1] : stt[0][1]);
  }
  STAMP(20, "c", "x flag");
  u32x2 z1p[2][7];
  {
    unsigned bw[7];
#pragma unroll
    for (int j = 0; j < 7; ++j) bw[j] = tr_addr(lds0 + L_B, 2 * j + 2 * (tn & 1), true, lane);
    f32x4 v1[7], v2[7]; f32x2 rs[2];
    auto tables = [&](int half) {
#pragma unroll
      for (int i = 0; i < 2; ++i) rs[i] = lds_ld<f32x2>(lds0 + T_RS + (32 * wid + 16 * i + i16) * 8);
#pragma unroll
      for (int j = 0; j < 7; ++j) { v1[j] = lds_ld<f32x4>(lds0 + T_V1 + (half * 112 + 16 * j + 4 * g) * 4); v2[j] = lds_ld<f32x4>(lds0 + T_V2 + (half * 112 + 16 * j + 4 * g) * 4); }
    };
    auto act = [&](f32x4 (&acc)[2][7]) {
#pragma unroll
      for (int i = 0; i < 2; ++i)
#pragma unroll
        for (int j = 0; j < 7; ++j) acc[i][j] = acc[i][j] * rs[i][0] + (v1[j] * rs[i][1] + v2[j]);
      SB();
      gelu_t<7>(acc[0]); gelu_t<7>(acc[1]);
    };
    {
      f32x4 acc[2][7];
      kloop<2, 7, B_WIDE, 16, 99, false>(acc, aaddr, bw, STREF(21, "c", "P4a start"), [&] { tables(0); });
      STAMP(22, "c", "P4a loop end");
      act(acc);
      u32x2 up[2][7];
#pragma unroll
      for (int i = 0; i < 2; ++i)
#pragma unroll
        for (int j = 0; j < 7; ++j) up[i][j] = pk4(acc[i][j]);
      const s16x8 ones = {0x3f80, 0x3f80, 0x3f80, 0x3f80, 0x3f80, 0x3f80, 0x3f80, 0x3f80};
      f32x4 s1[2], s2[2];
#pragma unroll
      for (int i = 0; i < 2; ++i) { s1[i] = (f32x4)(0.f); s2[i] = (f32x4)(0.f); }
#pragma unroll
      for (int j = 0; j < 4; ++j)
#pragma unroll
        for (int i = 0; i < 2; ++i) {
          const u32x4 w = {up[i][2 * j][0], up[i][2 * j][1], j < 3 ? up[i][j < 3 ? 2 * j + 1 : 0][0] : 0u, j < 3 ? up[i][j < 3 ? 2 * j + 1 : 0][1] : 0u};
          const s16x8 f = __builtin_bit_cast(s16x8, w);
          s1[i] = mfma16(ones, f, s1[i]); s2[i] = mfma16(f, f, s2[i]);
        }
      {
        auto diag = [&](f32x4 d) { return (i16 & 2) ? ((i16 & 1) ? d[3] : d[2]) : ((i16 & 1) ? d[1] : d[0]); };
        const float q0 = diag(s2[0]), q1 = diag(s2[1]), m0 = s1[0][0] * (1.f / 112.f), m1 = s1[1][0] * (1.f / 112.f);
        if (g == (i16 >> 2)) {
          const unsigned o = WS_ZS + (unsigned)((tm * 16 + tn) * 128 + 32 * wid + i16) * 16;
          put2(c, o, m0, fmaxf(fmaf(-m0, s1[0][0], q0), 0.f)); put2(c, o + 256, m1, fmaxf(fmaf(-m1, s1[1][0], q1), 0.f));
        }
      }
#pragma unroll
      for (int i = 0; i < 2; ++i)
#pragma unroll
        for (int j = 0; j < 7; ++j) lds_st<u32x2>(lds0 + L_STG + (32 * wid + 16 * i + i16) * 224 + j * 32 + g * 8, up[i][j]);
      lbar();
    }
    STAMP(23, "c", "P4a epi end");
    {
      f32x4 acc[2][7];
      kloop<2, 7, B_WIDE, 16, 99, false>(acc, aaddr, bw, tdummy, [&] { tables(1); });
      STAMP(24, "c", "P4b loop end");
      act(acc);
#pragma unroll
      for (int i = 0; i < 2; ++i)
#pragma unroll
        for (int j = 0; j < 7; ++j) { z1p[i][j] = pk4(acc[i][j]); asm volatile("" : "+v"(z1p[i][j])); }
    }
  }
  STAMP(25, "c", "P4b epi end");
  {
    unsigned bk[2];
    a_addrs(bk, lds0 + L_B, 0, lane);
    f32x4 acc[2][7]; float bs[2];
    kloop<2, 7, B_NK, 32, 99, false>(acc, aaddr, bk, STREF(26, "c", "P5 start"), [&] {
      bs[0] = lds_ld<float>(lds0 + T_X + (32 * wid + i16) * 4); bs[1] = lds_ld<float>(lds0 + T_X + (32 * wid + 16 + i16) * 4);
    });
    STAMP(27, "c", "P5 loop end");
    const unsigned sy = lds0 + L_STG + wid * 7168;
#pragma unroll
    for (int i = 0; i < 2; ++i)
#pragma unroll
      for (int j = 0; j < 7; ++j) lds_st<u32x2>(sy + (16 * i + i16) * 224 + j * 32 + g * 8, pk4(unpk4(z1p[i][j]) * (acc[i][j] + bs[i])));
    wait_lgkm0();
    u32x4 v[7];
#pragma unroll
    for (int i = 0; i < 7; ++i) v[i] = lds_ld<u32x4>(sy + i * 1024 + lane * 16);
#pragma unroll
    for (int i = 0; i < 7; ++i) __builtin_amdgcn_raw_buffer_store_b128(v[i], c.ws, WS_YP + (unsigned)(tm * 16 + tn) * Y_A + wid * 7168 + i * 1024 + lane * 16, 0, 16);
    STAMP(28, "c", "y stores issued");
    wait_vm<0>(); arrive<4>(c, F_Y, tm, tn);
  }
  STAMP(29, "c", "y flag");
  {
    f32x4 acc[2][4]; u32x2 gw[4];
    {
      s16x8 fa[2][3][2]; s16x4 fl[2][3][4], fh[2][3][4], ta[2][2], tb[2][4];
      const unsigned a0 = lds0 + (32 * wid + i16) * 224 + g * 16;
      const unsigned db = 12288 - (g >> 1) * 1536, da = 192 + (g & 1) * 16 + (g >> 1) * 8 - g * 16;
      unsigned ca, cb[4], b0[4];
#pragma unroll
      for (int j = 0; j < 4; ++j) b0[j] = bn[j] - L_B + Y_B0;
#pragma unroll
      for (int i = 0; i < 2; ++i)
#pragma unroll
        for (int j = 0; j < 4; ++j) { acc[i][j] = (f32x4)(0.f); asm volatile("" : "+v"(acc[i][j])); }
      auto setb = [&](auto X, unsigned sa, unsigned sb) { constexpr int x = X.value;
        if constexpr (x == 0) { ca = a0 + sa; asm volatile("" : "+v"(ca)); }
        else if constexpr (x < 5) { cb[x - 1] = b0[x - 1] + sb; asm volatile("" : "+v"(cb[x - 1])); }
      };
      auto rd = [&](auto SET, auto R) { constexpr int set = SET.value, ks = R.value / 10, r = R.value % 10;
        if constexpr (ks < 3) {
          if constexpr (r == 0) fa[set][ks][0] = lds_ld<s16x8>(ca + ks * 64);
          else if constexpr (r == 9) fa[set][ks][1] = lds_ld<s16x8>(ca + ks * 64 + 16 * 224);
          else { constexpr int j = (r - 1) / 2; if constexpr ((r - 1) % 2 == 0) fl[set][ks][j] = trread(cb[j] + ks * 4096); else fh[set][ks][j] = trread(cb[j] + ks * 4096 + 512); }
        } else if constexpr (r < 2) ta[set][r] = lds_ld<s16x4>(ca + da + r * 16 * 224); else tb[set][r - 2] = trread(cb[r - 2] + db); };
      auto mm = [&](auto SET, auto Q) { constexpr int set = SET.value, ks = Q.value / 8, i = (Q.value % 8) / 4, j = Q.value % 4;
        if constexpr (ks < 3) { f32x4 t = acc[i][j]; const s16x8 x = __builtin_shufflevector(fl[set][ks][j], fh[set][ks][j], 0, 1, 2, 3, 4, 5, 6, 7), y = fa[set][ks][i];
          asm volatile("v_mfma_f32_16x16x32_bf16 %0, %1, %2, %0" : "+v"(t) : "v"(x), "v"(y)); acc[i][j] = t; }
        else { f32x4 t = acc[i][j]; const s16x4 x = tb[set][j], y = ta[set][i];
          asm volatile("v_mfma_f32_16x16x16_bf16 %0, %1, %2, %0" : "+v"(t) : "v"(x), "v"(y)); acc[i][j] = t; } };
      auto step = [&](auto C, int k) {
        unsigned sa = Y_A0 + (unsigned)mod3(k + 1) * Y_A, sb = (unsigned)mod3(k + 2) * Y_B; asm volatile("" : "+s"(sa), "+s"(sb));
        static_for<0, 32>([&](auto Q) { constexpr int q = Q.value, r0 = q < 27 ? q * 4 / 3 : 36, r1 = q < 27 ? (q + 1) * 4 / 3 : 36;
          mm(C, Q); SB();
          static_for<r0, r1>([&](auto R) { rd(IC<1 - C.value>{}, R); });
          if constexpr (q >= 27) setb(IC<q - 27>{}, sa, sb);
          SB(); }); };
      static_for<0, 5>([&](auto X) { setb(X, (unsigned)L_STG, 0u); });
      lbar();
      static_for<0, 36>([&](auto R) { rd(IC<0>{}, R); });
      static_for<0, 5>([&](auto X) { setb(X, (unsigned)Y_A0, (unsigned)Y_B); });
      SB();
      static_for<0, 32>([&](auto Q) { mm(IC<0>{}, Q); });
      bar();
      static_for<0, 36>([&](auto R) { rd(IC<1>{}, R); });
      SB();
      static_for<0, 5>([&](auto X) { setb(X, (unsigned)(Y_A0 + Y_A), 2u * Y_B); });
      for (int k = 1; k < 15; k += 2) {
        wait_lgkm0(); bar(); step(IC<1>{}, k);
        wait_lgkm0(); bar(); step(IC<0>{}, k + 1);
      }
      wait_lgkm0(); bar();
#pragma unroll
      for (int j = 0; j < 4; ++j) gw[j] = lds_ld<u32x2>(lds0 + T_GB + (16 * j + 4 * g) * 2);
      SB();
      static_for<0, 32>([&](auto Q) { mm(IC<1>{}, Q); });
      asm volatile("s_nop 7\n\ts_nop 7" : "+v"(acc[0][0]), "+v"(acc[0][1]), "+v"(acc[0][2]), "+v"(acc[0][3]), "+v"(acc[1][0]), "+v"(acc[1][1]), "+v"(acc[1][2]), "+v"(acc[1][3]));
    }
    STAMP(30, "c", "P6 loop end");
#pragma unroll
    for (int i = 0; i < 2; ++i)
#pragma unroll
      for (int j = 0; j < 4; ++j) lds_st<u32x2>(img(i, j), pk4(unpk4(x1p[i][j]) + unpk4(gw[j]) * acc[i][j]));
    wait_lgkm0();
    u32x4 v[4];
#pragma unroll
    for (int i = 0; i < 4; ++i) { const int row = i * 8 + (lane >> 3); v[i] = lds_ld<u32x4>(sg + row * 128 + (((lane & 7) ^ ((row >> 1) & 7)) * 16)); }
#pragma unroll
    for (int i = 0; i < 4; ++i) __builtin_amdgcn_raw_buffer_store_b128(v[i], mk_rsrc(p.out, S * D * 2), (unsigned)((tm * 128 + 32 * wid + i * 8 + (lane >> 3)) * D + 64 * tn + (lane & 7) * 8) * 2, 0, 17);
  }
}

extern "C" __global__ void fused_block_end() {}

extern "C" void kernel_launch(void* const* d_in, const int* in_sizes, int n_in, void* d_out, int out_size, void* d_ws, size_t ws_size, hipStream_t stream) {
  Params p;
  p.x = (const u16*)d_in[0]; p.wqkv = (const u16*)d_in[1]; p.wphi = (const u16*)d_in[2]; p.wo = (const u16*)d_in[3];
  p.win = (const u16*)d_in[5]; p.wsp = (const u16*)d_in[6]; p.bsp = (const u16*)d_in[7]; p.wout = (const u16*)d_in[8];
  p.c = (const u16*)d_in[9]; p.wmod = (const u16*)d_in[10]; p.out = (u16*)d_out; p.ws = (char*)d_ws;
  (void)hipFuncSetAttribute((const void*)fused_block, hipFuncAttributeMaxDynamicSharedMemorySize, L_TOTAL);
  hipLaunchKernelGGL(fused_block, dim3(256), dim3(512), L_TOTAL, stream, p);
}
```
